```python
import torch

def _softmax(x, axis=-1):
    return torch.softmax(x, dim=axis)
def _gelu(x):
    return torch.nn.functional.gelu(x, approximate="tanh")
def _silu(x):
    return torch.nn.functional.silu(x)
def _sigmoid(x):
    return torch.sigmoid(x)
def _layernorm(x, axis=-1, eps=1e-5):
    m = x.mean(dim=axis, keepdim=True)
    v = x.var(dim=axis, keepdim=True, unbiased=False)
    return (x - m) / torch.sqrt(v + eps)
def _scatter_add(tgt, idx, src, axis=0):
    return tgt.clone().index_add_(axis, idx, src)
def _segment_sum(src, seg, n):
    out = torch.zeros((n,) + src.shape[1:], dtype=src.dtype)
    return out.index_add_(0, seg, src)
def _pad(x, pw):
    flat = tuple(v for pair in reversed(pw) for v in pair)
    return torch.nn.functional.pad(x, flat)

kernel_name = 'parallel_sum__mha_block_cached__glu_gated_glu__decode'

def setup_inputs():

    _w = (lambda a: (lambda t: t.to(torch.bfloat16) if t.is_floating_point() else t)(torch.as_tensor(a)))
    def _A_setup():
        B, Sq, D = 1, 1, 1024
        H, Sk, Dh = 32, 65536, 128
        return {
            "x": _w((lambda *s: torch.randn(*s, dtype=torch.bfloat16) * 0.05)(B, Sq, D)),
            "k_cache": _w((lambda *s: torch.randn(*s, dtype=torch.bfloat16) * 3.2)(B, H, Sk, Dh)),
            "v_cache": _w((lambda *s: torch.randn(*s, dtype=torch.bfloat16) * 1.6)(B, H, Sk, Dh)),
            "wq": _w((lambda *s: torch.randn(*s, dtype=torch.bfloat16) * 3.2)(D, H * Dh) * (D ** -0.5)),
            "wo": _w((lambda *s: torch.randn(*s, dtype=torch.bfloat16) * 1.6)(H * Dh, D) * ((H * Dh) ** -0.5)),
        }
    _A_in = _A_setup()

    def _B_setup():
        B, S, D, F = 1, 1, 1024, 3584
        return {
            "x": _w((lambda *s: torch.randn(*s, dtype=torch.bfloat16) * 0.05)(B, S, D)),
            "w_gate": _w((lambda *s: torch.randn(*s, dtype=torch.bfloat16) * 0.05)(D, F)),
            "w_up": _w((lambda *s: torch.randn(*s, dtype=torch.bfloat16) * 0.05)(D, F)),
            "w_down": _w((lambda *s: torch.randn(*s, dtype=torch.bfloat16) * 0.05)(F, D)),
        }
    _B_in = _B_setup()

    return {
        "A_x": _A_in["x"],
        "A_k_cache": _A_in["k_cache"],
        "A_v_cache": _A_in["v_cache"],
        "A_wq": _A_in["wq"],
        "A_wo": _A_in["wo"],
        "B_x": _B_in["x"],
        "B_w_gate": _B_in["w_gate"],
        "B_w_up": _B_in["w_up"],
        "B_w_down": _B_in["w_down"],
    }

def reference(A_x, A_k_cache, A_v_cache, A_wq, A_wo, B_x, B_w_gate, B_w_up, B_w_down):
    def _A_ref(x, k_cache, v_cache, wq, wo):
        B, Sq, D = x.shape
        H, Dh = 32, 128
        q = (lambda x, *d: x.permute(*d))((x @ wq).reshape(B, Sq, H, Dh), 0, 2, 1, 3)
        scores = torch.einsum("bhqd,bhkd->bhqk", q, k_cache) * (Dh ** -0.5)
        p = _softmax(scores, axis=-1)
        o = torch.einsum("bhqk,bhkd->bhqd", p, v_cache)
        return (lambda x, *d: x.permute(*d))(o, 0, 2, 1, 3).reshape(B, Sq, H * Dh) @ wo

    def _B_ref(x, w_gate, w_up, w_down):
        g = torch.einsum("bsd,df->bsf", x, w_gate)
        u = torch.einsum("bsd,df->bsf", x, w_up)
        h = _sigmoid(g) * u
        return torch.einsum("bsf,fd->bsd", h, w_down)

    x = A_x
    xn = _layernorm(x, axis=-1)
    return x + _A_ref(xn, A_k_cache, A_v_cache, A_wq, A_wo) + _B_ref(xn, B_w_gate, B_w_up, B_w_down)
```

```cpp
#include <hip/hip_runtime.h>
#include <hip/hip_bf16.h>
#include <cstdint>
#include <type_traits>

#define RND 1
#define NSPLIT 8

using bf16x2 = __attribute__((ext_vector_type(2))) __bf16;
using u32x4 = __attribute__((ext_vector_type(4))) unsigned;
using u32x2 = __attribute__((ext_vector_type(2))) unsigned;
using f32x4 = __attribute__((ext_vector_type(4))) float;

constexpr int D = 1024, H = 32, SK = 65536, DH = 128, F = 3584;

__device__ __forceinline__ float dot2(unsigned k, unsigned q, float acc) {
  return __builtin_amdgcn_fdot2_f32_bf16(__builtin_bit_cast(bf16x2, k), __builtin_bit_cast(bf16x2, q), acc, false);
}
__device__ __forceinline__ unsigned cvtpk(float lo, float hi) {
  unsigned r; asm("v_cvt_pk_bf16_f32 %0, %1, %2" : "=v"(r) : "v"(lo), "v"(hi)); return r;
}
__device__ __forceinline__ float bflo(unsigned u) { return __builtin_bit_cast(float, u << 16); }
__device__ __forceinline__ float bfhi(unsigned u) { return __builtin_bit_cast(float, u & 0xffff0000u); }
__device__ __forceinline__ float rbf(float x) { return bflo(cvtpk(x, x)); }
template <int CTRL> __device__ __forceinline__ float dpp(float x) {
  return __builtin_bit_cast(float, __builtin_amdgcn_mov_dpp(__builtin_bit_cast(int, x), CTRL, 0xf, 0xf, true));
}
constexpr int XOR1 = 0xB1, XOR2 = 0x4E, XOR7 = 0x141, XOR8 = 0x128, ROR4 = 0x124;
__device__ __forceinline__ float xrow16_max(float x) {
  auto s = __builtin_amdgcn_permlane16_swap(__float_as_uint(x), __float_as_uint(x), false, false);
  x = fmaxf(__uint_as_float(s[0]), __uint_as_float(s[1]));
  auto t = __builtin_amdgcn_permlane32_swap(__float_as_uint(x), __float_as_uint(x), false, false);
  return fmaxf(__uint_as_float(t[0]), __uint_as_float(t[1]));
}
__device__ __forceinline__ float xrow16_sum(float x) {
  auto s = __builtin_amdgcn_permlane16_swap(__float_as_uint(x), __float_as_uint(x), false, false);
  x = __uint_as_float(s[0]) + __uint_as_float(s[1]);
  auto t = __builtin_amdgcn_permlane32_swap(__float_as_uint(x), __float_as_uint(x), false, false);
  return __uint_as_float(t[0]) + __uint_as_float(t[1]);
}
__device__ __forceinline__ float row16_sum(float x) {
  x += dpp<XOR1>(x); x += dpp<XOR2>(x); x += dpp<XOR7>(x); x += dpp<XOR8>(x); return x;
}
__device__ __forceinline__ float wave_sum(float x) { return xrow16_sum(row16_sum(x)); }

constexpr size_t WS_QP = 0;
constexpr size_t WS_GP = WS_QP + 8 * 4096;
constexpr size_t WS_UP = WS_GP + 8 * F;
constexpr size_t WS_PO = WS_UP + 8 * F;
constexpr size_t WS_ML = WS_PO + (size_t)H * NSPLIT * DH;
constexpr size_t WS_OP = WS_ML + (size_t)H * NSPLIT * 2;

__device__ __forceinline__ void layernorm_to_lds(const __hip_bfloat16* x, float* xn, float* red) {
  const int tid = threadIdx.x;
  u32x2 v = *reinterpret_cast<const u32x2*>(x + 4 * tid);
  float a0 = bflo(v[0]), a1 = bfhi(v[0]), a2 = bflo(v[1]), a3 = bfhi(v[1]);
  float s = wave_sum(a0 + a1 + a2 + a3);
  if ((tid & 63) == 0) red[tid >> 6] = s;
  __syncthreads();
  float mean = (red[0] + red[1] + red[2] + red[3]) * (1.f / D);
  float d0 = a0 - mean, d1 = a1 - mean, d2 = a2 - mean, d3 = a3 - mean;
  float q = wave_sum(d0 * d0 + d1 * d1 + d2 * d2 + d3 * d3);
  if ((tid & 63) == 0) red[4 + (tid >> 6)] = q;
  __syncthreads();
  float var = (red[4] + red[5] + red[6] + red[7]) * (1.f / D);
  if (RND >= 2) {
    float mb = rbf(mean), vb = rbf(var);
    float sd = rbf(sqrtf(rbf(vb + 1e-5f)));
    d0 = rbf(rbf(a0 - mb) / sd); d1 = rbf(rbf(a1 - mb) / sd); d2 = rbf(rbf(a2 - mb) / sd); d3 = rbf(rbf(a3 - mb) / sd);
  } else {
    float rs = 1.f / sqrtf(var + 1e-5f);
    d0 *= rs; d1 *= rs; d2 *= rs; d3 *= rs;
    if (RND >= 1) { d0 = rbf(d0); d1 = rbf(d1); d2 = rbf(d2); d3 = rbf(d3); }
  }
  *reinterpret_cast<f32x4*>(xn + 4 * tid) = f32x4{d0, d1, d2, d3};
  __syncthreads();
}

__device__ __forceinline__ void tile_gemv(const __hip_bfloat16* W, int ld, int r0, int c0, const float* vec, float* red  , float* out) {
  const int tid = threadIdx.x, rg = tid >> 4, c = tid & 15;
  u32x4 w[8];
#pragma unroll
  for (int i = 0; i < 8; ++i) w[i] = __builtin_nontemporal_load(reinterpret_cast<const u32x4*>(W + (size_t)(r0 + rg + 16 * i) * ld + c0 + 8 * c));
  float acc[8] = {0, 0, 0, 0, 0, 0, 0, 0};
#pragma unroll
  for (int i = 0; i < 8; ++i) {
    float xv = vec[rg + 16 * i];
#pragma unroll
    for (int k = 0; k < 4; ++k) { acc[2 * k] = fmaf(xv, bflo(w[i][k]), acc[2 * k]); acc[2 * k + 1] = fmaf(xv, bfhi(w[i][k]), acc[2 * k + 1]); }
  }
#pragma unroll
  for (int k = 0; k < 8; ++k) acc[k] = xrow16_sum(acc[k]);
  const int wave = tid >> 6;
  if ((tid & 63) < 16) {
    *reinterpret_cast<f32x4*>(red + wave * 128 + 8 * c) = f32x4{acc[0], acc[1], acc[2], acc[3]};
    *reinterpret_cast<f32x4*>(red + wave * 128 + 8 * c + 4) = f32x4{acc[4], acc[5], acc[6], acc[7]};
  }
  __syncthreads();
  if (tid < 128) out[tid] = red[tid] + red[128 + tid] + red[256 + tid] + red[384 + tid];
}

__global__ void __launch_bounds__(256) k_pre(const __hip_bfloat16* x, const __hip_bfloat16* wq, const __hip_bfloat16* wg, const __hip_bfloat16* wu, float* ws) {
  __shared__ __attribute__((aligned(16))) float xn[D];
  __shared__ __attribute__((aligned(16))) float red[512];
  layernorm_to_lds(x, xn, red);
  int b = blockIdx.x;
  const int ks = b & 7; b >>= 3;
  if (b < 32) tile_gemv(wq, 4096, ks * 128, b * 128, xn + ks * 128, red, ws + WS_QP + ks * 4096 + b * 128);
  else if (b < 60) tile_gemv(wg, F, ks * 128, (b - 32) * 128, xn + ks * 128, red, ws + WS_GP + ks * F + (b - 32) * 128);
  else tile_gemv(wu, F, ks * 128, (b - 60) * 128, xn + ks * 128, red, ws + WS_UP + ks * F + (b - 60) * 128);
}

constexpr int U = 16;
__global__ void __launch_bounds__(256) k_attn(const __hip_bfloat16* kc, const __hip_bfloat16* vc, const __hip_bfloat16* wd, float* ws) {
  __shared__ __attribute__((aligned(16))) float lds[1024];
  const int tid = threadIdx.x, lane = tid & 63, wave = __builtin_amdgcn_readfirstlane(tid >> 6), r = lane >> 4, c = lane & 15;
  int b = blockIdx.x;
  if (b >= H * NSPLIT) {
    b -= H * NSPLIT;
    const int rb = b >> 3, cb = b & 7;
    if (tid < 128) {
      float g = 0, u = 0;
#pragma unroll
      for (int s = 0; s < 8; ++s) { g += ws[WS_GP + s * F + rb * 128 + tid]; u += ws[WS_UP + s * F + rb * 128 + tid]; }
      float hh;
      if (RND >= 2) { g = rbf(g); u = rbf(u); hh = rbf(rbf(1.f / (1.f + __expf(-g))) * u); }
      else hh = u / (1.f + __expf(-g));
      lds[512 + tid] = hh;
    }
    __syncthreads();
    tile_gemv(wd, D, rb * 128, cb * 128, lds + 512, lds, ws + WS_OP + (size_t)(32 + rb) * D + cb * 128);
    return;
  }
  const int h = b / NSPLIT, s = b % NSPLIT;
  constexpr int KEYS = SK / NSPLIT;
  constexpr int NB = KEYS / (4 * 4 * U);
  if (tid < 128) {
    float q = 0;
#pragma unroll
    for (int ss = 0; ss < 8; ++ss) q += ws[WS_QP + ss * 4096 + h * 128 + tid];
    lds[tid] = q;
  }
  __syncthreads();
  unsigned qh[4], ql[4];
#pragma unroll
  for (int k = 0; k < 4; ++k) {
    float a = lds[8 * c + 2 * k], bb = lds[8 * c + 2 * k + 1];
    qh[k] = cvtpk(a, bb);
    ql[k] = cvtpk(a - bflo(qh[k]), bb - bfhi(qh[k]));
  }
  __syncthreads();
  const size_t base = ((size_t)h * SK + (size_t)s * KEYS) * DH + (size_t)wave * (4 * U * DH) + r * DH + 8 * c;
  const u32x4* kp = reinterpret_cast<const u32x4*>(kc + base);
  const u32x4* vp = reinterpret_cast<const u32x4*>(vc + base);
  constexpr int TS = 4 * DH / 8;
  constexpr int BS = 4 * 4 * U * DH / 8;
  constexpr float SC = 0.088388347648318440f, L2E = 1.4426950408889634f;
  float m = -INFINITY, l = 0.f, acc[8] = {0, 0, 0, 0, 0, 0, 0, 0};
  u32x4 Kn[U], Vn[U];
#pragma unroll
  for (int t = 0; t < U; ++t) Kn[t] = __builtin_nontemporal_load(kp + t * TS);
#pragma unroll
  for (int t = 0; t < U; ++t) Vn[t] = __builtin_nontemporal_load(vp + t * TS);
  auto body = [&](int in, auto more_) {
    constexpr bool more = decltype(more_)::value;
    float x[U];
#pragma unroll
    for (int t = 0; t < U; ++t) {
      float d = 0.f;
#pragma unroll
      for (int k = 0; k < 4; ++k) d = dot2(Kn[t][k], qh[k], d);
      if (RND == 0) {
#pragma unroll
        for (int k = 0; k < 4; ++k) d = dot2(Kn[t][k], ql[k], d);
      }
      x[t] = d;
    }
    __builtin_amdgcn_sched_barrier(0);
    if constexpr (more) {
#pragma unroll
    for (int t = 0; t < U; ++t) Kn[t] = __builtin_nontemporal_load(kp + (size_t)in * BS + t * TS);
    }
    __builtin_amdgcn_sched_barrier(0);
#define TR_STEP(HALF, CTRL, BIT) _Pragma("unroll") for (int hh = 0; hh < (HALF); ++hh) { \
      const float lo_ = x[hh], hi_ = x[hh + (HALF)]; const float t1_ = lo_ + dpp<CTRL>(lo_), t2_ = hi_ + dpp<CTRL>(hi_); \
      x[hh] = (c & (BIT)) ? t2_ : t1_; }
    TR_STEP(8, XOR8, 8) TR_STEP(4, XOR7, 4) TR_STEP(2, XOR2, 2) TR_STEP(1, XOR1, 1)
#undef TR_STEP
    float sc;
    if (RND >= 2) sc = rbf(rbf(x[0]) * rbf(SC)) * L2E; else sc = x[0] * (SC * L2E);
    float pm = sc;
    pm = fmaxf(pm, dpp<XOR1>(pm)); pm = fmaxf(pm, dpp<XOR2>(pm)); pm = fmaxf(pm, dpp<XOR7>(pm)); pm = fmaxf(pm, dpp<XOR8>(pm));
    pm = xrow16_max(pm);
    const float mn = fmaxf(m, pm);
    const float alpha = __builtin_amdgcn_exp2f(m - mn);
    const float p = __builtin_amdgcn_exp2f(sc - mn);
    l = fmaf(l, alpha, p); m = mn;
#pragma unroll
    for (int k = 0; k < 8; ++k) acc[k] *= alpha;
#define PV(T) { const float pt = dpp<0x150 + (T)>(p); _Pragma("unroll") for (int k = 0; k < 4; ++k) { \
      acc[2 * k] = fmaf(pt, bflo(Vn[T][k]), acc[2 * k]); acc[2 * k + 1] = fmaf(pt, bfhi(Vn[T][k]), acc[2 * k + 1]); } }
    PV(0) PV(1) PV(2) PV(3) PV(4) PV(5) PV(6) PV(7) PV(8) PV(9) PV(10) PV(11) PV(12) PV(13) PV(14) PV(15)
#undef PV
    __builtin_amdgcn_sched_barrier(0);
    if constexpr (more) {
#pragma unroll
    for (int t = 0; t < U; ++t) Vn[t] = __builtin_nontemporal_load(vp + (size_t)in * BS + t * TS);
    }
    __builtin_amdgcn_sched_barrier(0);
  };
  for (int i = 1; i < NB; ++i) body(i, std::true_type{});
  body(0, std::false_type{});
#pragma unroll
  for (int k = 0; k < 8; ++k) acc[k] = xrow16_sum(acc[k]);
  l = wave_sum(l);
  float* rec = lds + wave * 132;
  if (r == 0) {
    *reinterpret_cast<f32x4*>(rec + 4 + 8 * c) = f32x4{acc[0], acc[1], acc[2], acc[3]};
    *reinterpret_cast<f32x4*>(rec + 4 + 8 * c + 4) = f32x4{acc[4], acc[5], acc[6], acc[7]};
    if (c == 0) { rec[0] = m; rec[1] = l; }
  }
  __syncthreads();
  if (tid < 128) {
    float M = fmaxf(fmaxf(lds[0], lds[132]), fmaxf(lds[264], lds[396]));
    float L = 0.f, o = 0.f;
#pragma unroll
    for (int w = 0; w < 4; ++w) { float wt = __builtin_amdgcn_exp2f(lds[w * 132] - M); L = fmaf(wt, lds[w * 132 + 1], L); o = fmaf(wt, lds[w * 132 + 4 + tid], o); }
    ws[WS_PO + (size_t)(h * NSPLIT + s) * DH + tid] = o;
    if (tid == 0) { ws[WS_ML + (h * NSPLIT + s) * 2] = M; ws[WS_ML + (h * NSPLIT + s) * 2 + 1] = L; }
  }
}

__global__ void __launch_bounds__(256) k_post(const __hip_bfloat16* wo, float* ws) {
  __shared__ __attribute__((aligned(16))) float lds[1024];
  const int tid = threadIdx.x, h = blockIdx.x >> 3, cb = blockIdx.x & 7;
  if (tid < 128) {
    float M = -INFINITY;
#pragma unroll
    for (int s = 0; s < NSPLIT; ++s) M = fmaxf(M, ws[WS_ML + (h * NSPLIT + s) * 2]);
    float L = 0.f, o = 0.f;
#pragma unroll
    for (int s = 0; s < NSPLIT; ++s) {
      float wt = __builtin_amdgcn_exp2f(ws[WS_ML + (h * NSPLIT + s) * 2] - M);
      L = fmaf(wt, ws[WS_ML + (h * NSPLIT + s) * 2 + 1], L);
      o = fmaf(wt, ws[WS_PO + (size_t)(h * NSPLIT + s) * DH + tid], o);
    }
    o = o / L;
    if (RND >= 2) o = rbf(o);
    lds[512 + tid] = o;
  }
  __syncthreads();
  tile_gemv(wo, D, h * 128, cb * 128, lds + 512, lds, ws + WS_OP + (size_t)h * D + cb * 128);
}

__global__ void __launch_bounds__(256) k_fin(const __hip_bfloat16* x, const float* ws, __hip_bfloat16* out) {
  const int j = blockIdx.x * 256 + threadIdx.x;
  float a = 0.f, f = 0.f;
#pragma unroll
  for (int i = 0; i < 32; ++i) a += ws[WS_OP + (size_t)i * D + j];
#pragma unroll
  for (int i = 32; i < 60; ++i) f += ws[WS_OP + (size_t)i * D + j];
  float xv = __bfloat162float(x[j]);
  float o;
  if (RND >= 2) o = rbf(xv + rbf(a)) + rbf(f); else o = xv + a + f;
  out[j] = __float2bfloat16(o);
}

extern "C" void kernel_launch(void* const* d_in, const int* in_sizes, int n_in, void* d_out, int out_size, void* d_ws, size_t ws_size, hipStream_t stream) {
  const __hip_bfloat16* x = (const __hip_bfloat16*)d_in[0];
  const __hip_bfloat16* kc = (const __hip_bfloat16*)d_in[1];
  const __hip_bfloat16* vc = (const __hip_bfloat16*)d_in[2];
  const __hip_bfloat16* wq = (const __hip_bfloat16*)d_in[3];
  const __hip_bfloat16* wo = (const __hip_bfloat16*)d_in[4];
  const __hip_bfloat16* wg = (const __hip_bfloat16*)d_in[6];
  const __hip_bfloat16* wu = (const __hip_bfloat16*)d_in[7];
  const __hip_bfloat16* wd = (const __hip_bfloat16*)d_in[8];
  float* ws = (float*)d_ws;
  k_pre<<<88 * 8, 256, 0, stream>>>(x, wq, wg, wu, ws);
  k_attn<<<H * NSPLIT + 28 * 8, 256, 0, stream>>>(kc, vc, wd, ws);
  k_post<<<256, 256, 0, stream>>>(wo, ws);
  k_fin<<<4, 256, 0, stream>>>(x, ws, (__hip_bfloat16*)d_out);
}
```

```cpp
#define PMODE 0
#include <hip/hip_runtime.h>
#include <hip/hip_bf16.h>
#include <cstdint>
#include <type_traits>

#define RND 2
#define NSPLIT 8

using bf16x2 = __attribute__((ext_vector_type(2))) __bf16;
using u32x4 = __attribute__((ext_vector_type(4))) unsigned;
using u32x2 = __attribute__((ext_vector_type(2))) unsigned;
using f32x4 = __attribute__((ext_vector_type(4))) float;

constexpr int D = 1024, H = 32, SK = 65536, DH = 128, F = 3584;

__device__ __forceinline__ float dot2(unsigned k, unsigned q, float acc) {
  return __builtin_amdgcn_fdot2_f32_bf16(__builtin_bit_cast(bf16x2, k), __builtin_bit_cast(bf16x2, q), acc, false);
}
__device__ __forceinline__ unsigned cvtpk(float lo, float hi) {
  unsigned r; asm("v_cvt_pk_bf16_f32 %0, %1, %2" : "=v"(r) : "v"(lo), "v"(hi)); return r;
}
__device__ __forceinline__ float bflo(unsigned u) { return __builtin_bit_cast(float, u << 16); }
__device__ __forceinline__ float bfhi(unsigned u) { return __builtin_bit_cast(float, u & 0xffff0000u); }
__device__ __forceinline__ float rbf(float x) { return bflo(cvtpk(x, x)); }
template <int CTRL> __device__ __forceinline__ float dpp(float x) {
  return __builtin_bit_cast(float, __builtin_amdgcn_mov_dpp(__builtin_bit_cast(int, x), CTRL, 0xf, 0xf, true));
}
constexpr int XOR1 = 0xB1, XOR2 = 0x4E, XOR7 = 0x141, XOR8 = 0x128, ROR4 = 0x124;
__device__ __forceinline__ float xrow16_max(float x) {
  auto s = __builtin_amdgcn_permlane16_swap(__float_as_uint(x), __float_as_uint(x), false, false);
  x = fmaxf(__uint_as_float(s[0]), __uint_as_float(s[1]));
  auto t = __builtin_amdgcn_permlane32_swap(__float_as_uint(x), __float_as_uint(x), false, false);
  return fmaxf(__uint_as_float(t[0]), __uint_as_float(t[1]));
}
__device__ __forceinline__ float xrow16_sum(float x) {
  auto s = __builtin_amdgcn_permlane16_swap(__float_as_uint(x), __float_as_uint(x), false, false);
  x = __uint_as_float(s[0]) + __uint_as_float(s[1]);
  auto t = __builtin_amdgcn_permlane32_swap(__float_as_uint(x), __float_as_uint(x), false, false);
  return __uint_as_float(t[0]) + __uint_as_float(t[1]);
}
__device__ __forceinline__ float row16_sum(float x) {
  x += dpp<XOR1>(x); x += dpp<XOR2>(x); x += dpp<XOR7>(x); x += dpp<XOR8>(x); return x;
}
__device__ __forceinline__ float wave_sum(float x) { return xrow16_sum(row16_sum(x)); }

constexpr size_t WS_QP = 0;
constexpr size_t WS_GP = WS_QP + 8 * 4096;
constexpr size_t WS_UP = WS_GP + 8 * F;
constexpr size_t WS_PO = WS_UP + 8 * F;
constexpr size_t WS_ML = WS_PO + (size_t)H * NSPLIT * DH;
constexpr size_t WS_OP = WS_ML + (size_t)H * NSPLIT * 2;

__device__ __forceinline__ void layernorm_to_lds(const __hip_bfloat16* x, float* xn, float* red) {
  const int tid = threadIdx.x;
  u32x2 v = *reinterpret_cast<const u32x2*>(x + 4 * tid);
  float a0 = bflo(v[0]), a1 = bfhi(v[0]), a2 = bflo(v[1]), a3 = bfhi(v[1]);
  float s = wave_sum(a0 + a1 + a2 + a3);
  if ((tid & 63) == 0) red[tid >> 6] = s;
  __syncthreads();
  float mean = (red[0] + red[1] + red[2] + red[3]) * (1.f / D);
  float d0 = a0 - mean, d1 = a1 - mean, d2 = a2 - mean, d3 = a3 - mean;
  float q = wave_sum(d0 * d0 + d1 * d1 + d2 * d2 + d3 * d3);
  if ((tid & 63) == 0) red[4 + (tid >> 6)] = q;
  __syncthreads();
  float var = (red[4] + red[5] + red[6] + red[7]) * (1.f / D);
  if (RND >= 2) {
    float mb = rbf(mean), vb = rbf(var);
    float sd = rbf(sqrtf(rbf(vb + 1e-5f)));
    d0 = rbf(rbf(a0 - mb) / sd); d1 = rbf(rbf(a1 - mb) / sd); d2 = rbf(rbf(a2 - mb) / sd); d3 = rbf(rbf(a3 - mb) / sd);
  } else {
    float rs = 1.f / sqrtf(var + 1e-5f);
    d0 *= rs; d1 *= rs; d2 *= rs; d3 *= rs;
    if (RND >= 1) { d0 = rbf(d0); d1 = rbf(d1); d2 = rbf(d2); d3 = rbf(d3); }
  }
  *reinterpret_cast<f32x4*>(xn + 4 * tid) = f32x4{d0, d1, d2, d3};
  __syncthreads();
}

__device__ __forceinline__ void tile_gemv(const __hip_bfloat16* W, int ld, int r0, int c0, const float* vec, float* red  , float* out) {
  const int tid = threadIdx.x, rg = tid >> 4, c = tid & 15;
  u32x4 w[8];
#pragma unroll
  for (int i = 0; i < 8; ++i) w[i] = __builtin_nontemporal_load(reinterpret_cast<const u32x4*>(W + (size_t)(r0 + rg + 16 * i) * ld + c0 + 8 * c));
  float acc[8] = {0, 0, 0, 0, 0, 0, 0, 0};
#pragma unroll
  for (int i = 0; i < 8; ++i) {
    float xv = vec[rg + 16 * i];
#pragma unroll
    for (int k = 0; k < 4; ++k) { acc[2 * k] = fmaf(xv, bflo(w[i][k]), acc[2 * k]); acc[2 * k + 1] = fmaf(xv, bfhi(w[i][k]), acc[2 * k + 1]); }
  }
#pragma unroll
  for (int k = 0; k < 8; ++k) acc[k] = xrow16_sum(acc[k]);
  const int wave = tid >> 6;
  if ((tid & 63) < 16) {
    *reinterpret_cast<f32x4*>(red + wave * 128 + 8 * c) = f32x4{acc[0], acc[1], acc[2], acc[3]};
    *reinterpret_cast<f32x4*>(red + wave * 128 + 8 * c + 4) = f32x4{acc[4], acc[5], acc[6], acc[7]};
  }
  __syncthreads();
  if (tid < 128) out[tid] = red[tid] + red[128 + tid] + red[256 + tid] + red[384 + tid];
}

__global__ void __launch_bounds__(256) k_pre(const __hip_bfloat16* x, const __hip_bfloat16* wq, const __hip_bfloat16* wg, const __hip_bfloat16* wu, float* ws) {
  __shared__ __attribute__((aligned(16))) float xn[D];
  __shared__ __attribute__((aligned(16))) float red[512];
  layernorm_to_lds(x, xn, red);
  int b = blockIdx.x;
  const int ks = b & 7; b >>= 3;
  if (b < 32) tile_gemv(wq, 4096, ks * 128, b * 128, xn + ks * 128, red, ws + WS_QP + ks * 4096 + b * 128);
  else if (b < 60) tile_gemv(wg, F, ks * 128, (b - 32) * 128, xn + ks * 128, red, ws + WS_GP + ks * F + (b - 32) * 128);
  else tile_gemv(wu, F, ks * 128, (b - 60) * 128, xn + ks * 128, red, ws + WS_UP + ks * F + (b - 60) * 128);
}

constexpr int U = 16;
__global__ void __launch_bounds__(256) k_attn(const __hip_bfloat16* kc, const __hip_bfloat16* vc, const __hip_bfloat16* wd, float* ws) {
  __shared__ __attribute__((aligned(16))) float lds[1024];
  const int tid = threadIdx.x, lane = tid & 63, wave = __builtin_amdgcn_readfirstlane(tid >> 6), r = lane >> 4, c = lane & 15;
  int b = blockIdx.x;
  if (b >= H * NSPLIT) {
    b -= H * NSPLIT;
    const int rb = b >> 3, cb = b & 7;
    if (tid < 128) {
      float g = 0, u = 0;
#pragma unroll
      for (int s = 0; s < 8; ++s) { g += ws[WS_GP + s * F + rb * 128 + tid]; u += ws[WS_UP + s * F + rb * 128 + tid]; }
      float hh;
      if (RND >= 2) { g = rbf(g); u = rbf(u); hh = rbf(rbf(1.f / (1.f + __expf(-g))) * u); }
      else hh = u / (1.f + __expf(-g));
      lds[512 + tid] = hh;
    }
    __syncthreads();
    tile_gemv(wd, D, rb * 128, cb * 128, lds + 512, lds, ws + WS_OP + (size_t)(32 + rb) * D + cb * 128);
    return;
  }
  const int h = b / NSPLIT, s = b % NSPLIT;
  constexpr int KEYS = SK / NSPLIT;
  constexpr int NB = KEYS / (4 * 4 * U);
  if (tid < 128) {
    float q = 0;
#pragma unroll
    for (int ss = 0; ss < 8; ++ss) q += ws[WS_QP + ss * 4096 + h * 128 + tid];
    lds[tid] = q;
  }
  __syncthreads();
  unsigned qh[4], ql[4];
#pragma unroll
  for (int k = 0; k < 4; ++k) {
    float a = lds[8 * c + 2 * k], bb = lds[8 * c + 2 * k + 1];
    qh[k] = cvtpk(a, bb);
    ql[k] = cvtpk(a - bflo(qh[k]), bb - bfhi(qh[k]));
  }
  __syncthreads();
  const size_t base = ((size_t)h * SK + (size_t)s * KEYS) * DH + (size_t)wave * (4 * U * DH) + r * DH + 8 * c;
  const u32x4* kp = reinterpret_cast<const u32x4*>(kc + base);
  const u32x4* vp = reinterpret_cast<const u32x4*>(vc + base);
  constexpr int TS = 4 * DH / 8;
  constexpr int BS = 4 * 4 * U * DH / 8;
  constexpr float SC = 0.088388347648318440f, L2E = 1.4426950408889634f;
  float m = -INFINITY, l = 0.f, acc[8] = {0, 0, 0, 0, 0, 0, 0, 0};
  u32x4 Kn[U], Vn[U];
#pragma unroll
  for (int t = 0; t < U; ++t) Kn[t] = __builtin_nontemporal_load(kp + t * TS);
#pragma unroll
  for (int t = 0; t < U; ++t) Vn[t] = __builtin_nontemporal_load(vp + t * TS);
  auto body = [&](int in, auto more_) {
    constexpr bool more = decltype(more_)::value;
    float x[U];
#pragma unroll
    for (int t = 0; t < U; ++t) {
      float d = 0.f;
#pragma unroll
      for (int k = 0; k < 4; ++k) d = dot2(Kn[t][k], qh[k], d);
      if (RND == 0) {
#pragma unroll
        for (int k = 0; k < 4; ++k) d = dot2(Kn[t][k], ql[k], d);
      }
      x[t] = d;
    }
    __builtin_amdgcn_sched_barrier(0);
    if constexpr (more) {
#pragma unroll
    for (int t = 0; t < U; ++t) Kn[t] = __builtin_nontemporal_load(kp + (size_t)in * BS + t * TS);
    }
    __builtin_amdgcn_sched_barrier(0);
#define TR_STEP(HALF, CTRL, BIT) _Pragma("unroll") for (int hh = 0; hh < (HALF); ++hh) { \
      const float lo_ = x[hh], hi_ = x[hh + (HALF)]; const float t1_ = lo_ + dpp<CTRL>(lo_), t2_ = hi_ + dpp<CTRL>(hi_); \
      x[hh] = (c & (BIT)) ? t2_ : t1_; }
    TR_STEP(8, XOR8, 8) TR_STEP(4, XOR7, 4) TR_STEP(2, XOR2, 2) TR_STEP(1, XOR1, 1)
#undef TR_STEP
    float sc;
    if (RND >= 2) sc = rbf(rbf(x[0]) * rbf(SC)) * L2E; else sc = x[0] * (SC * L2E);
    float pm = sc;
    pm = fmaxf(pm, dpp<XOR1>(pm)); pm = fmaxf(pm, dpp<XOR2>(pm)); pm = fmaxf(pm, dpp<XOR7>(pm)); pm = fmaxf(pm, dpp<XOR8>(pm));
    pm = xrow16_max(pm);
    const float mn = fmaxf(m, pm);
    const float alpha = __builtin_amdgcn_exp2f(m - mn);
    const float p = __builtin_amdgcn_exp2f(sc - mn);
    l = fmaf(l, alpha, p); m = mn;
#pragma unroll
    for (int k = 0; k < 8; ++k) acc[k] *= alpha;
#define PV(T) { const float pt = dpp<0x150 + (T)>(p); _Pragma("unroll") for (int k = 0; k < 4; ++k) { \
      acc[2 * k] = fmaf(pt, bflo(Vn[T][k]), acc[2 * k]); acc[2 * k + 1] = fmaf(pt, bfhi(Vn[T][k]), acc[2 * k + 1]); } }
    PV(0) PV(1) PV(2) PV(3) PV(4) PV(5) PV(6) PV(7) PV(8) PV(9) PV(10) PV(11) PV(12) PV(13) PV(14) PV(15)
#undef PV
    __builtin_amdgcn_sched_barrier(0);
    if constexpr (more) {
#pragma unroll
    for (int t = 0; t < U; ++t) Vn[t] = __builtin_nontemporal_load(vp + (size_t)in * BS + t * TS);
    }
    __builtin_amdgcn_sched_barrier(0);
  };
  for (int i = 1; i < NB; ++i) body(i, std::true_type{});
  body(0, std::false_type{});
#pragma unroll
  for (int k = 0; k < 8; ++k) acc[k] = xrow16_sum(acc[k]);
  l = wave_sum(l);
  float* rec = lds + wave * 132;
  if (r == 0) {
    *reinterpret_cast<f32x4*>(rec + 4 + 8 * c) = f32x4{acc[0], acc[1], acc[2], acc[3]};
    *reinterpret_cast<f32x4*>(rec + 4 + 8 * c + 4) = f32x4{acc[4], acc[5], acc[6], acc[7]};
    if (c == 0) { rec[0] = m; rec[1] = l; }
  }
  __syncthreads();
  if (tid < 128) {
    float M = fmaxf(fmaxf(lds[0], lds[132]), fmaxf(lds[264], lds[396]));
    float L = 0.f, o = 0.f;
#pragma unroll
    for (int w = 0; w < 4; ++w) { float wt = __builtin_amdgcn_exp2f(lds[w * 132] - M); L = fmaf(wt, lds[w * 132 + 1], L); o = fmaf(wt, lds[w * 132 + 4 + tid], o); }
    ws[WS_PO + (size_t)(h * NSPLIT + s) * DH + tid] = o;
    if (tid == 0) { ws[WS_ML + (h * NSPLIT + s) * 2] = M; ws[WS_ML + (h * NSPLIT + s) * 2 + 1] = L; }
  }
}

__global__ void __launch_bounds__(256) k_post(const __hip_bfloat16* wo, float* ws) {
  __shared__ __attribute__((aligned(16))) float lds[1024];
  const int tid = threadIdx.x, h = blockIdx.x >> 3, cb = blockIdx.x & 7;
  if (tid < 128) {
    float M = -INFINITY;
#pragma unroll
    for (int s = 0; s < NSPLIT; ++s) M = fmaxf(M, ws[WS_ML + (h * NSPLIT + s) * 2]);
    float L = 0.f, o = 0.f;
#pragma unroll
    for (int s = 0; s < NSPLIT; ++s) {
      float wt = __builtin_amdgcn_exp2f(ws[WS_ML + (h * NSPLIT + s) * 2] - M);
      L = fmaf(wt, ws[WS_ML + (h * NSPLIT + s) * 2 + 1], L);
      o = fmaf(wt, ws[WS_PO + (size_t)(h * NSPLIT + s) * DH + tid], o);
    }
    o = o / L;
    if (RND >= 2) o = rbf(o);
    lds[512 + tid] = o;
  }
  __syncthreads();
  tile_gemv(wo, D, h * 128, cb * 128, lds + 512, lds, ws + WS_OP + (size_t)h * D + cb * 128);
}

__global__ void __launch_bounds__(256) k_fin(const __hip_bfloat16* x, const float* ws, __hip_bfloat16* out) {
  const int j = blockIdx.x * 256 + threadIdx.x;
  float a = 0.f, f = 0.f;
#pragma unroll
  for (int i = 0; i < 32; ++i) a += ws[WS_OP + (size_t)i * D + j];
#pragma unroll
  for (int i = 32; i < 60; ++i) f += ws[WS_OP + (size_t)i * D + j];
  float xv = __bfloat162float(x[j]);
  float o;
  if (RND >= 2) o = rbf(xv + rbf(a)) + rbf(f); else o = xv + a + f;
  out[j] = __float2bfloat16(o);
}

__device__ __forceinline__ unsigned tload(const char* p) {
  unsigned long long t0, t1; unsigned v;
  asm volatile("s_memtime %0\n s_waitcnt lgkmcnt(0)\n global_load_dword %2, %3, off nt\n s_waitcnt vmcnt(0)\n s_memtime %1\n s_waitcnt lgkmcnt(0)" : "=&s"(t0), "=&s"(t1), "=&v"(v) : "v"(p) : "memory");
  return (unsigned)(t1 - t0);
}
__device__ __forceinline__ int probe_page(const char* p) {
  tload(p);
  int sc = 0;
  for (int r = 0; r < 5; ++r) { unsigned e = tload(p + 1024 + 1024 * r); unsigned o = tload(p + 8192 + 1024 + 1024 * r); sc += e < o ? 1 : -1; }
  return sc > 0;
}
__global__ void k_probe(const char* K, const char* V, unsigned* fl) {
  if (threadIdx.x != 0) return;
  const int j = blockIdx.x;
  int pk = probe_page(K + ((size_t)j << 21) + 65536);
  int pv = probe_page(V + ((size_t)j << 21) + 65536);
  int p0 = probe_page(K + 131072 + (size_t)(j >> 3) * 16384 );
  int m1 = pk != pv, m2 = (pk ^ p0) != ((j & 1) ^ (j >> 7));
  fl[j] = PMODE == 1 ? m1 : (PMODE == 2 ? m2 : 0);
}
__global__ void k_sleep(const unsigned* fl) {
  unsigned v = fl[threadIdx.x];
  int n = __syncthreads_count(v);
  if (threadIdx.x == 0) { unsigned long long t0 = __builtin_amdgcn_s_memrealtime(); while (__builtin_amdgcn_s_memrealtime() - t0 < (unsigned long long)n * 100) __builtin_amdgcn_s_sleep(1); }
}

extern "C" void kernel_launch(void* const* d_in, const int* in_sizes, int n_in, void* d_out, int out_size, void* d_ws, size_t ws_size, hipStream_t stream) {
  const __hip_bfloat16* x = (const __hip_bfloat16*)d_in[0];
  const __hip_bfloat16* kc = (const __hip_bfloat16*)d_in[1];
  const __hip_bfloat16* vc = (const __hip_bfloat16*)d_in[2];
  const __hip_bfloat16* wq = (const __hip_bfloat16*)d_in[3];
  const __hip_bfloat16* wo = (const __hip_bfloat16*)d_in[4];
  const __hip_bfloat16* wg = (const __hip_bfloat16*)d_in[6];
  const __hip_bfloat16* wu = (const __hip_bfloat16*)d_in[7];
  const __hip_bfloat16* wd = (const __hip_bfloat16*)d_in[8];
  float* ws = (float*)d_ws;
  k_probe<<<256, 64, 0, stream>>>((const char*)kc, (const char*)vc, (unsigned*)d_ws + (64 << 20));
  k_sleep<<<1, 256, 0, stream>>>((unsigned*)d_ws + (64 << 20));
  k_pre<<<88 * 8, 256, 0, stream>>>(x, wq, wg, wu, ws);
  k_attn<<<H * NSPLIT + 28 * 8, 256, 0, stream>>>(kc, vc, wd, ws);
  k_post<<<256, 256, 0, stream>>>(wo, ws);
  k_fin<<<4, 256, 0, stream>>>(x, ws, (__hip_bfloat16*)d_out);
}
```

```cpp
#include <hip/hip_runtime.h>
#include <hip/hip_bf16.h>
#include <cstdint>
#include <type_traits>

#define CS 56

using bf16x2 = __attribute__((ext_vector_type(2))) __bf16;
using u32x4 = __attribute__((ext_vector_type(4))) unsigned;
using u32x2 = __attribute__((ext_vector_type(2))) unsigned;
using f32x4 = __attribute__((ext_vector_type(4))) float;
using f32x2 = __attribute__((ext_vector_type(2))) float;
typedef __attribute__((address_space(1))) unsigned long long gu64;
typedef __attribute__((address_space(1))) unsigned gu32;
typedef __amdgpu_buffer_rsrc_t rsrc_t;

constexpr int D = 1024, H = 32, SK = 65536, DH = 128, F = 3584, NFB = F / 128;
constexpr int NCH = SK / 32;
constexpr int DYN0 = CS * 32;
constexpr unsigned SPIN_CAP = 1u << 17;

constexpr unsigned WS_EPOCH = 0;
#define CSTR 256
constexpr unsigned WS_CNT = 8u << 20;
constexpr unsigned WS_QG = 16384;
constexpr unsigned WS_GU = WS_QG + H * 8 * 1024;
constexpr unsigned WS_H1 = WS_GU + NFB * 8 * 2048;
constexpr unsigned H1_STRIDE = 1280;
constexpr unsigned WS_PA = WS_H1 + H * 8 * H1_STRIDE;
constexpr unsigned WS_PF = WS_PA + 8 * H * 1024;
constexpr unsigned WS_END = WS_PF + 8 * H * 1024;

#define SB() __builtin_amdgcn_sched_barrier(0)
#define PIN(x) asm volatile("" : "+v"(x))
#define PIN8(a) asm volatile("" : "+v"(a[0]), "+v"(a[1]), "+v"(a[2]), "+v"(a[3]), "+v"(a[4]), "+v"(a[5]), "+v"(a[6]), "+v"(a[7]))
__device__ __forceinline__ void wg_barrier() { asm volatile("s_waitcnt lgkmcnt(0)\n\ts_barrier" ::: "memory"); }

__device__ __forceinline__ float dot2(unsigned a, unsigned b, float acc) {
  return __builtin_amdgcn_fdot2_f32_bf16(__builtin_bit_cast(bf16x2, a), __builtin_bit_cast(bf16x2, b), acc, false);
}
__device__ __forceinline__ unsigned cvtpk(float lo, float hi) {
  unsigned r; asm("v_cvt_pk_bf16_f32 %0, %1, %2" : "=v"(r) : "v"(lo), "v"(hi)); return r;
}
__device__ __forceinline__ float bflo(unsigned u) { return __builtin_bit_cast(float, u << 16); }
__device__ __forceinline__ float bfhi(unsigned u) { return __builtin_bit_cast(float, u & 0xffff0000u); }
__device__ __forceinline__ float rbf(float x) { return bflo(cvtpk(x, x)); }
__device__ __forceinline__ float u2f(unsigned u) { return __builtin_bit_cast(float, u); }
__device__ __forceinline__ unsigned f2u(float f) { return __builtin_bit_cast(unsigned, f); }
template <int CTRL> __device__ __forceinline__ float dpp(float x) {
  return __builtin_bit_cast(float, __builtin_amdgcn_mov_dpp(__builtin_bit_cast(int, x), CTRL, 0xf, 0xf, true));
}
constexpr int XOR1 = 0xB1, XOR2 = 0x4E, XOR7 = 0x141, XOR8 = 0x128, ROR4 = 0x124;
__device__ __forceinline__ float swap32_add(float a, float b) {
  auto t = __builtin_amdgcn_permlane32_swap(f2u(a), f2u(b), false, false); return u2f(t[0]) + u2f(t[1]);
}
__device__ __forceinline__ float swap16_add(float a, float b) {
  auto t = __builtin_amdgcn_permlane16_swap(f2u(a), f2u(b), false, false); return u2f(t[0]) + u2f(t[1]);
}
__device__ __forceinline__ float xrow_sum(float x) { x = swap16_add(x, x); return swap32_add(x, x); }
__device__ __forceinline__ float xrow_max(float x) {
  auto s = __builtin_amdgcn_permlane16_swap(f2u(x), f2u(x), false, false); x = fmaxf(u2f(s[0]), u2f(s[1]));
  auto t = __builtin_amdgcn_permlane32_swap(f2u(x), f2u(x), false, false); return fmaxf(u2f(t[0]), u2f(t[1]));
}
__device__ __forceinline__ float row_sum(float x) { x += dpp<XOR1>(x); x += dpp<XOR2>(x); x += dpp<XOR7>(x); x += dpp<XOR8>(x); return x; }
__device__ __forceinline__ float wave_sum(float x) { return xrow_sum(row_sum(x)); }
__device__ __forceinline__ f32x2 rows_reduce(const float (&a)[8]) {
  const float y0 = swap32_add(a[0], a[4]), y1 = swap32_add(a[1], a[5]), y2 = swap32_add(a[2], a[6]), y3 = swap32_add(a[3], a[7]);
  return f32x2{swap16_add(y0, y2), swap16_add(y1, y3)};
}
__device__ __forceinline__ unsigned plo(unsigned w0, unsigned w1) { return __builtin_amdgcn_perm(w1, w0, 0x05040100u); }
__device__ __forceinline__ unsigned phi(unsigned w0, unsigned w1) { return __builtin_amdgcn_perm(w1, w0, 0x07060302u); }
__device__ __forceinline__ void mac_rows(float (&acc)[8], u32x4 w0, u32x4 w1, unsigned xp) {
#pragma unroll
  for (int k = 0; k < 4; ++k) { acc[2 * k] = dot2(plo(w0[k], w1[k]), xp, acc[2 * k]); acc[2 * k + 1] = dot2(phi(w0[k], w1[k]), xp, acc[2 * k + 1]); }
}
__device__ __forceinline__ rsrc_t mkrs(const void* p, unsigned bytes) { return __builtin_amdgcn_make_buffer_rsrc(const_cast<void*>(p), 0, bytes, 0x00020000); }
__device__ __forceinline__ u32x4 ldnt(rsrc_t rs, unsigned voff, unsigned soff) { return __builtin_amdgcn_raw_buffer_load_b128(rs, voff, soff, 2); }
__device__ __forceinline__ u32x4 gload(rsrc_t rs, unsigned voff, unsigned soff) { return __builtin_amdgcn_raw_buffer_load_b128(rs, voff, soff, 16); }
__device__ __forceinline__ void gstore(rsrc_t rs, unsigned voff, unsigned soff, float v0, float v1, unsigned tA, unsigned tB) {
  __builtin_amdgcn_raw_buffer_store_b128(u32x4{f2u(v0), tA, f2u(v1), tB}, rs, voff + soff, 0, 16);
}
struct LN { float mb, sd; };
__device__ __forceinline__ LN ln_stats(u32x4 a, u32x4 b) {
  float v[16];
#pragma unroll
  for (int k = 0; k < 4; ++k) { v[2 * k] = bflo(a[k]); v[2 * k + 1] = bfhi(a[k]); v[8 + 2 * k] = bflo(b[k]); v[9 + 2 * k] = bfhi(b[k]); }
  float s = 0.f;
#pragma unroll
  for (int k = 0; k < 16; ++k) s += v[k];
  const float mean = wave_sum(s) * (1.f / D);
  float q = 0.f;
#pragma unroll
  for (int k = 0; k < 16; ++k) { const float d = v[k] - mean; q = fmaf(d, d, q); }
  const float var = wave_sum(q) * (1.f / D);
  return LN{rbf(mean), rbf(sqrtf(rbf(rbf(var) + 1e-5f)))};
}
__device__ __forceinline__ unsigned ln_pair(unsigned u, LN p) { return cvtpk(rbf(bflo(u) - p.mb) / p.sd, rbf(bfhi(u) - p.mb) / p.sd); }

__global__ void __launch_bounds__(320, 2) fused_decode(const __hip_bfloat16* __restrict__ x, const __hip_bfloat16* __restrict__ kc, const __hip_bfloat16* __restrict__ vc,
                                                    const __hip_bfloat16* __restrict__ wq, const __hip_bfloat16* __restrict__ wo, const __hip_bfloat16* __restrict__ wg,
                                                    const __hip_bfloat16* __restrict__ wu, const __hip_bfloat16* __restrict__ wd, __hip_bfloat16* __restrict__ out, char* ws) {
  __shared__ __attribute__((aligned(16))) float part[4][128];
  __shared__ __attribute__((aligned(16))) float ml[4][2];
  __shared__ __attribute__((aligned(16))) unsigned vecb[64];
  __shared__ __attribute__((aligned(16))) unsigned hb[64];
  const int tid = threadIdx.x, lane = tid & 63, wave = __builtin_amdgcn_readfirstlane(tid >> 6), r = lane >> 4, c = lane & 15;
  const int h = blockIdx.x >> 3, s = blockIdx.x & 7;
#define STAMP(i)
#define SSTAMP(i)
  const u32x4 xa = reinterpret_cast<const u32x4*>(x)[2 * lane], xb = reinterpret_cast<const u32x4*>(x)[2 * lane + 1];

  if (wave < 4) {
    const int row0 = 32 * wave + 8 * r;
    const u32x4 xr = *reinterpret_cast<const u32x4*>(x + 128 * s + row0);
    SB();
    SSTAMP(24);
    u32x4 W[8], WO[8], Kn[8], Vn[8];
    {
      const rsrc_t rq = mkrs(wq, D * H * DH * 2), ro = mkrs(wo, D * H * DH * 2);
      const unsigned vq = ((128 * s + row0) * (H * DH) + 128 * h + 8 * c) * 2, vo = ((128 * h + row0) * D + 128 * s + 8 * c) * 2;
#pragma unroll
      for (int i = 0; i < 8; ++i) { W[i] = ldnt(rq, vq, i * (H * DH * 2)); SB(); }
      SSTAMP(25);
#pragma unroll
      for (int i = 0; i < 8; ++i) { WO[i] = ldnt(ro, vo, i * (D * 2)); SB(); }
    }
    const rsrc_t rk = mkrs(kc + (size_t)h * (SK * DH), SK * DH * 2), rv = mkrs(vc + (size_t)h * (SK * DH), SK * DH * 2);
    const unsigned lo16 = lane * 16;
    const int c0 = s * (CS * 4) + wave;
#define KV(R, CH, T) ldnt(R, lo16 + ((T) & 3) * 1024, (CH) * 8192 + ((T) >> 2) * 4096)
#pragma unroll
    for (int t = 0; t < 8; ++t) { Kn[t] = KV(rk, c0, t); SB(); }
#pragma unroll
    for (int t = 0; t < 8; ++t) { Vn[t] = KV(rv, c0, t); SB(); }
    SSTAMP(23);
    {
      const LN ln = ln_stats(xa, xb);
      SSTAMP(16);
      float a[8] = {0, 0, 0, 0, 0, 0, 0, 0};
#pragma unroll
      for (int j = 0; j < 4; ++j) mac_rows(a, W[2 * j], W[2 * j + 1], ln_pair(xr[j], ln));
      SSTAMP(17);
      *reinterpret_cast<f32x2*>(&part[wave][8 * c + 2 * r]) = rows_reduce(a);
    }
    wg_barrier();
    SB(); PIN8(WO); SB();
    unsigned wol[16], woh[16];
#pragma unroll
    for (int j = 0; j < 4; ++j)
#pragma unroll
      for (int k = 0; k < 4; ++k) { wol[4 * j + k] = plo(WO[2 * j][k], WO[2 * j + 1][k]); woh[4 * j + k] = phi(WO[2 * j][k], WO[2 * j + 1][k]); PIN(wol[4 * j + k]); PIN(woh[4 * j + k]); }
    SSTAMP(18);
    wg_barrier();
    const u32x4 q = *reinterpret_cast<const u32x4*>(&vecb[4 * c]);

    constexpr float L2E = 1.4426950408889634f;
    const float scb = rbf(0.088388347648318440f);
    float m = -INFINITY, l = 0.f;
    f32x2 acc[4] = {{0, 0}, {0, 0}, {0, 0}, {0, 0}};
    auto step = [&](int next, auto more_) {
      constexpr bool more = decltype(more_)::value;
      float xs[8];
#pragma unroll
      for (int t = 0; t < 8; ++t) {
        float d = dot2(Kn[t][0], q[0], 0.f); d = dot2(Kn[t][1], q[1], d); d = dot2(Kn[t][2], q[2], d); d = dot2(Kn[t][3], q[3], d);
        xs[t] = d; PIN(xs[t]); SB();
        if constexpr (more) { Kn[t] = KV(rk, next, t); SB(); }
      }
#define TR_STEP(HALF, CTRL, BIT) _Pragma("unroll") for (int hh = 0; hh < (HALF); ++hh) { \
      const float t1_ = xs[hh] + dpp<CTRL>(xs[hh]), t2_ = xs[hh + (HALF)] + dpp<CTRL>(xs[hh + (HALF)]); xs[hh] = (c & (BIT)) ? t2_ : t1_; }
      TR_STEP(4, XOR8, 8) TR_STEP(2, XOR7, 4) TR_STEP(1, XOR2, 2)
#undef TR_STEP
      const float dotf = xs[0] + dpp<XOR1>(xs[0]);
      const float sc = rbf(rbf(dotf) * scb) * L2E;
      float pm = fmaxf(sc, dpp<XOR2>(sc)); pm = fmaxf(pm, dpp<XOR7>(pm)); pm = fmaxf(pm, dpp<XOR8>(pm));
      const float mn = fmaxf(m, pm);
      const float alpha = __builtin_amdgcn_exp2f(m - mn);
      const float p = __builtin_amdgcn_exp2f(sc - mn);
      l = fmaf(l, alpha, p); m = mn;
#pragma unroll
      for (int k = 0; k < 4; ++k) acc[k] *= alpha;
#define PV(T) { const float pt = dpp<0x150 + 2 * (T)>(p); const f32x2 p2 = {pt, pt}; \
      _Pragma("unroll") for (int k = 0; k < 4; ++k) acc[k] = __builtin_elementwise_fma(p2, f32x2{bflo(Vn[T][k]), bfhi(Vn[T][k])}, acc[k]); \
      PIN(acc[0]); PIN(acc[1]); PIN(acc[2]); PIN(acc[3]); SB(); \
      if constexpr (more) { Vn[T] = KV(rv, next, T); SB(); } }
      PV(0) PV(1) PV(2) PV(3) PV(4) PV(5) PV(6) PV(7)
#undef PV
    };
    for (int i = 1; i < CS - 1; ++i) step(c0 + 4 * i, std::true_type{});
    int ndyn = 0;
    {
      const rsrc_t crs = __builtin_amdgcn_make_buffer_rsrc(ws + WS_CNT + h * CSTR, 0, 4, 0x00020000);
      const unsigned coff = lane == 0 ? 0u : 0x80000000u;
      int next = c0 + 4 * (CS - 1);
      while (true) {
        const int tk = __builtin_amdgcn_raw_ptr_buffer_atomic_add_i32(1, crs, coff, 0, 0);
        SB();
        step(next, std::true_type{});
        const unsigned t = __builtin_amdgcn_readfirstlane(tk);
        if (t >= (unsigned)(NCH - DYN0)) break;
        next = DYN0 + (int)t;
        ++ndyn;
      }
    }
    step(0, std::false_type{});
    {
      const float M = xrow_max(m), e = __builtin_amdgcn_exp2f(m - M);
      const float a[8] = {acc[0][0] * e, acc[0][1] * e, acc[1][0] * e, acc[1][1] * e, acc[2][0] * e, acc[2][1] * e, acc[3][0] * e, acc[3][1] * e};
      *reinterpret_cast<f32x2*>(&part[wave][8 * c + 2 * r]) = rows_reduce(a);
      const float L = wave_sum(l * e) * 0.5f;
      if (lane == 0) *reinterpret_cast<f32x2*>(&ml[wave][0]) = f32x2{M, L};
    }
    wg_barrier();
    wg_barrier();
    {
      const u32x4 ob = *reinterpret_cast<const u32x4*>(&vecb[16 * wave + 4 * r]);
      float a[8] = {0, 0, 0, 0, 0, 0, 0, 0};
#pragma unroll
      for (int j = 0; j < 4; ++j)
#pragma unroll
        for (int k = 0; k < 4; ++k) { a[2 * k] = dot2(wol[4 * j + k], ob[j], a[2 * k]); a[2 * k + 1] = dot2(woh[4 * j + k], ob[j], a[2 * k + 1]); }
      *reinterpret_cast<f32x2*>(&part[wave][8 * c + 2 * r]) = rows_reduce(a);
    }
    wg_barrier();
    return;
  }

  __builtin_amdgcn_s_setprio(3);
  const rsrc_t rs = __builtin_amdgcn_make_buffer_rsrc(ws, 0, WS_END, 0x00020000);
  const unsigned long long ep = __hip_atomic_load((gu64*)(ws + WS_EPOCH), __ATOMIC_RELAXED, __HIP_MEMORY_SCOPE_AGENT);
  const bool ffn = h < NFB;
  u32x4 xh[4];
#pragma unroll
  for (int j = 0; j < 4; ++j) xh[j] = *reinterpret_cast<const u32x4*>(x + 128 * s + 32 * r + 8 * j);
  const unsigned xo = *reinterpret_cast<const unsigned*>(x + 128 * s + 4 * h + 2 * (lane & 1));
  if (s == 0 && lane == 0) __hip_atomic_store((gu32*)(ws + WS_CNT + h * CSTR), 0u, __ATOMIC_RELAXED, __HIP_MEMORY_SCOPE_AGENT);
  const unsigned tA = __builtin_amdgcn_readfirstlane((unsigned)ep) + 1u, tB = __builtin_amdgcn_readfirstlane((unsigned)(ep >> 32)) + 1u;
  {
    const LN ln = ln_stats(xa, xb);
#pragma unroll
    for (int j = 0; j < 4; ++j)
#pragma unroll
      for (int k = 0; k < 4; ++k) { xh[j][k] = ln_pair(xh[j][k], ln); }
    PIN(xh[0]); PIN(xh[1]); PIN(xh[2]); PIN(xh[3]);
  }
  asm volatile("s_waitcnt vmcnt(0)" ::: "memory");
  const unsigned lo16 = lane * 16, pair16 = (4 * c + r) * 16;
  auto sum4 = [&]() {
    const f32x2 a = *reinterpret_cast<const f32x2*>(&part[0][2 * lane]), b = *reinterpret_cast<const f32x2*>(&part[1][2 * lane]);
    const f32x2 cc = *reinterpret_cast<const f32x2*>(&part[2][2 * lane]), d = *reinterpret_cast<const f32x2*>(&part[3][2 * lane]);
    return (a + b) + (cc + d);
  };
  auto good = [&](u32x4 g) { return ((g[1] ^ tA) | (g[3] ^ tB)) == 0u; };
  wg_barrier();
  STAMP(1);
  { const f32x2 v = sum4(); gstore(rs, lo16, WS_QG + (h * 8 + s) * 1024, v[0], v[1], tA, tB); }
  SB();
  u32x4 P[8];
  int npoll = 0;
  auto q_issue = [&]() {
    asm volatile("" ::: "memory");
#pragma unroll
    for (int p = 0; p < 8; ++p) { P[p] = gload(rs, lo16, WS_QG + (h * 8 + p) * 1024); SB(); }
    ++npoll;
  };
  auto q_check = [&](bool force) {
    bool ok = true;
#pragma unroll
    for (int p = 0; p < 8; ++p) ok &= good(P[p]);
    if (!__all(ok) && !force) return false;
    float a = 0.f, b = 0.f;
#pragma unroll
    for (int p = 0; p < 8; ++p) { a += u2f(P[p][0]); b += u2f(P[p][2]); }
    vecb[lane] = cvtpk(a, b);
    wg_barrier();
    __builtin_amdgcn_s_setprio(0);
    STAMP(2);
    return true;
  };
  float fsum0, fsum1;
  if (ffn) {
    u32x4 G[32];
    const rsrc_t rg = mkrs(wg, D * F * 2), ru = mkrs(wu, D * F * 2), rd = mkrs(wd, D * F * 2);
    const unsigned vg = ((128 * s + 32 * r) * F + 128 * h + 8 * c) * 2, vd = ((128 * h + 32 * r) * D + 128 * s + 8 * c) * 2;
#pragma unroll
    for (int i = 0; i < 16; ++i) { G[i] = ldnt(rg, vg, i * (F * 2)); SB(); }
    q_issue();
#pragma unroll
    for (int i = 16; i < 32; ++i) { G[i] = ldnt(rg, vg, i * (F * 2)); SB(); }
    bool ok = q_check(false);
    float ag[8] = {0, 0, 0, 0, 0, 0, 0, 0}, au[8] = {0, 0, 0, 0, 0, 0, 0, 0};
    if (!ok) q_issue();
    SB();
#pragma unroll
    for (int i = 0; i < 16; i += 2) { mac_rows(ag, G[i], G[i + 1], xh[i >> 3][(i >> 1) & 3]); PIN8(ag); SB(); G[i] = ldnt(ru, vg, i * (F * 2)); G[i + 1] = ldnt(ru, vg, (i + 1) * (F * 2)); SB(); }
    if (!ok) { ok = q_check(false); if (!ok) q_issue(); }
    SB();
#pragma unroll
    for (int i = 16; i < 32; i += 2) { mac_rows(ag, G[i], G[i + 1], xh[i >> 3][(i >> 1) & 3]); PIN8(ag); SB(); G[i] = ldnt(ru, vg, i * (F * 2)); G[i + 1] = ldnt(ru, vg, (i + 1) * (F * 2)); SB(); }
    if (!ok) {
      for (unsigned spin = 0; !q_check(spin >= SPIN_CAP); ++spin) q_issue();
    }
    SB();
#pragma unroll
    for (int i = 0; i < 32; i += 2) { mac_rows(au, G[i], G[i + 1], xh[i >> 3][(i >> 1) & 3]); PIN8(au); SB(); G[i] = ldnt(rd, vd, i * (D * 2)); G[i + 1] = ldnt(rd, vd, (i + 1) * (D * 2)); SB(); }
    {
      const f32x2 g2 = rows_reduce(ag), u2 = rows_reduce(au);
      gstore(rs, pair16, WS_GU + (h * 8 + s) * 2048, g2[0], g2[1], tA, tB);
      gstore(rs, pair16, WS_GU + (h * 8 + s) * 2048 + 1024, u2[0], u2[1], tA, tB);
    }
    float g0, g1, u0, u1;
    for (unsigned spin = 0;; ++spin) {
      asm volatile("" ::: "memory");
      bool okg = true; g0 = g1 = u0 = u1 = 0.f;
#pragma unroll
      for (int half = 0; half < 2; ++half) {
#pragma unroll
        for (int p = 0; p < 8; ++p) P[p] = gload(rs, lo16, WS_GU + (h * 8 + 4 * half + (p >> 1)) * 2048 + (p & 1) * 1024);
#pragma unroll
        for (int p = 0; p < 8; p += 2) { okg &= good(P[p]); okg &= good(P[p + 1]); g0 += u2f(P[p][0]); g1 += u2f(P[p][2]); u0 += u2f(P[p + 1][0]); u1 += u2f(P[p + 1][2]); }
      }
      if (__all(okg) || spin >= SPIN_CAP) break;
      __builtin_amdgcn_s_sleep(8);
    }
    g0 = rbf(g0); g1 = rbf(g1);
    hb[lane] = cvtpk(rbf(1.f / (1.f + __expf(-g0))) * rbf(u0), rbf(1.f / (1.f + __expf(-g1))) * rbf(u1));
    float ad[8] = {0, 0, 0, 0, 0, 0, 0, 0};
#pragma unroll
    for (int j = 0; j < 4; ++j) {
      const u32x4 hv = *reinterpret_cast<const u32x4*>(&hb[16 * r + 4 * j]);
#pragma unroll
      for (int k = 0; k < 4; ++k) mac_rows(ad, G[8 * j + 2 * k], G[8 * j + 2 * k + 1], hv[k]);
    }
    const f32x2 d2 = rows_reduce(ad);
    gstore(rs, pair16, WS_PF + (s * H + h) * 1024, d2[0], d2[1], tA, tB);
  } else {
    q_issue();
    for (unsigned spin = 0; !q_check(spin >= SPIN_CAP); ++spin) q_issue();
  }
  const unsigned coloff = (lane >> 1) * 1024 + (2 * h + (lane & 1)) * 16;
  auto col_reduce = [&](float v) { v += dpp<XOR2>(v); v += dpp<XOR8>(v); v += dpp<ROR4>(v); return xrow_sum(v); };
  for (unsigned spin = 0;; ++spin) {
    asm volatile("" ::: "memory");
    const u32x4 g = gload(rs, coloff, WS_PF + s * H * 1024);
    const bool in = lane < 2 * NFB;
    fsum0 = in ? u2f(g[0]) : 0.f; fsum1 = in ? u2f(g[2]) : 0.f;
    if (__all(good(g) | !in) || spin >= SPIN_CAP) break;
    __builtin_amdgcn_s_sleep(8);
  }
  fsum0 = rbf(col_reduce(fsum0)); fsum1 = rbf(col_reduce(fsum1));
  STAMP(3);
  __builtin_amdgcn_s_setprio(3);

  wg_barrier();
  {
    const f32x4 m01 = *reinterpret_cast<const f32x4*>(&ml[0][0]), m23 = *reinterpret_cast<const f32x4*>(&ml[2][0]);
    const float M = fmaxf(fmaxf(m01[0], m01[2]), fmaxf(m23[0], m23[2]));
    const float e0 = __builtin_amdgcn_exp2f(m01[0] - M), e1 = __builtin_amdgcn_exp2f(m01[2] - M), e2 = __builtin_amdgcn_exp2f(m23[0] - M), e3 = __builtin_amdgcn_exp2f(m23[2] - M);
    const f32x2 a = *reinterpret_cast<const f32x2*>(&part[0][2 * lane]), b = *reinterpret_cast<const f32x2*>(&part[1][2 * lane]);
    const f32x2 cc = *reinterpret_cast<const f32x2*>(&part[2][2 * lane]), d = *reinterpret_cast<const f32x2*>(&part[3][2 * lane]);
    const f32x2 o = (a * e0 + b * e1) + (cc * e2 + d * e3);
    const float L = (m01[1] * e0 + m01[3] * e1) + (m23[1] * e2 + m23[3] * e3);
    gstore(rs, lo16, WS_H1 + (h * 8 + s) * H1_STRIDE, o[0], o[1], tA, tB);
    gstore(rs, lane == 0 ? 1024u : 0x80000000u, WS_H1 + (h * 8 + s) * H1_STRIDE, M, L, tA, tB);
  }
  STAMP(8);
  {
    u32x4 g;
    for (unsigned spin = 0;; ++spin) {
      asm volatile("" ::: "memory");
      g = gload(rs, (lane & 7) * H1_STRIDE + 1024, WS_H1 + h * 8 * H1_STRIDE);
#pragma unroll
      for (int p = 0; p < 8; ++p) P[p] = gload(rs, lo16, WS_H1 + (h * 8 + p) * H1_STRIDE);
      bool ok = good(g);
#pragma unroll
      for (int p = 0; p < 8; ++p) ok &= good(P[p]);
      if (__all(ok) || spin >= SPIN_CAP) break;
    }
    const float mp = u2f(g[0]);
    float M = fmaxf(mp, dpp<XOR1>(mp)); M = fmaxf(M, dpp<XOR2>(M)); M = fmaxf(M, dpp<XOR7>(M));
    const float e = __builtin_amdgcn_exp2f(mp - M);
    float L = e * u2f(g[2]); L += dpp<XOR1>(L); L += dpp<XOR2>(L); L += dpp<XOR7>(L);
    float o0 = 0.f, o1 = 0.f;
#pragma unroll
    for (int p = 0; p < 8; ++p) { const float ep_ = u2f(__builtin_amdgcn_readlane(f2u(e), p)); o0 = fmaf(ep_, u2f(P[p][0]), o0); o1 = fmaf(ep_, u2f(P[p][2]), o1); }
    vecb[lane] = cvtpk(o0 / L, o1 / L);
  }
  wg_barrier();
  STAMP(9);
  wg_barrier();
  { const f32x2 v = sum4(); gstore(rs, lo16, WS_PA + (s * H + h) * 1024, v[0], v[1], tA, tB); }
  STAMP(10);
  float a0, a1;
  for (unsigned spin = 0;; ++spin) {
    asm volatile("" ::: "memory");
    const u32x4 g = gload(rs, coloff, WS_PA + s * H * 1024);
    a0 = u2f(g[0]); a1 = u2f(g[2]);
    if (__all(good(g)) || spin >= SPIN_CAP) break;
  }
  a0 = rbf(col_reduce(a0)); a1 = rbf(col_reduce(a1));
  const unsigned res = cvtpk(rbf(bflo(xo) + a0) + fsum0, rbf(bfhi(xo) + a1) + fsum1);
  if (lane < 2) *reinterpret_cast<unsigned*>(out + 128 * s + 4 * h + 2 * lane) = res;
  if (blockIdx.x == 0 && lane == 0) __hip_atomic_store((gu64*)(ws + WS_EPOCH), ((unsigned long long)tB << 32) | tA, __ATOMIC_RELAXED, __HIP_MEMORY_SCOPE_AGENT);
}

extern "C" void kernel_launch(void* const* d_in, const int* in_sizes, int n_in, void* d_out, int out_size, void* d_ws, size_t ws_size, hipStream_t stream) {
  fused_decode<<<256, 320, 0, stream>>>((const __hip_bfloat16*)d_in[0], (const __hip_bfloat16*)d_in[1], (const __hip_bfloat16*)d_in[2], (const __hip_bfloat16*)d_in[3],
                                        (const __hip_bfloat16*)d_in[4], (const __hip_bfloat16*)d_in[6], (const __hip_bfloat16*)d_in[7], (const __hip_bfloat16*)d_in[8],
                                        (__hip_bfloat16*)d_out, (char*)d_ws);
}
```

```cpp
#include <hip/hip_runtime.h>
#include <hip/hip_bf16.h>
#include <cstdint>
#include <type_traits>

#define RND 2
#define NSPLIT 8

using bf16x2 = __attribute__((ext_vector_type(2))) __bf16;
using u32x4 = __attribute__((ext_vector_type(4))) unsigned;
using u32x2 = __attribute__((ext_vector_type(2))) unsigned;
using f32x4 = __attribute__((ext_vector_type(4))) float;

constexpr int D = 1024, H = 32, SK = 65536, DH = 128, F = 3584;

__device__ __forceinline__ float dot2(unsigned k, unsigned q, float acc) {
  return __builtin_amdgcn_fdot2_f32_bf16(__builtin_bit_cast(bf16x2, k), __builtin_bit_cast(bf16x2, q), acc, false);
}
__device__ __forceinline__ unsigned cvtpk(float lo, float hi) {
  unsigned r; asm("v_cvt_pk_bf16_f32 %0, %1, %2" : "=v"(r) : "v"(lo), "v"(hi)); return r;
}
__device__ __forceinline__ float bflo(unsigned u) { return __builtin_bit_cast(float, u << 16); }
__device__ __forceinline__ float bfhi(unsigned u) { return __builtin_bit_cast(float, u & 0xffff0000u); }
__device__ __forceinline__ float rbf(float x) { return bflo(cvtpk(x, x)); }
template <int CTRL> __device__ __forceinline__ float dpp(float x) {
  return __builtin_bit_cast(float, __builtin_amdgcn_mov_dpp(__builtin_bit_cast(int, x), CTRL, 0xf, 0xf, true));
}
constexpr int XOR1 = 0xB1, XOR2 = 0x4E, XOR7 = 0x141, XOR8 = 0x128, ROR4 = 0x124;
__device__ __forceinline__ float xrow16_max(float x) {
  auto s = __builtin_amdgcn_permlane16_swap(__float_as_uint(x), __float_as_uint(x), false, false);
  x = fmaxf(__uint_as_float(s[0]), __uint_as_float(s[1]));
  auto t = __builtin_amdgcn_permlane32_swap(__float_as_uint(x), __float_as_uint(x), false, false);
  return fmaxf(__uint_as_float(t[0]), __uint_as_float(t[1]));
}
__device__ __forceinline__ float xrow16_sum(float x) {
  auto s = __builtin_amdgcn_permlane16_swap(__float_as_uint(x), __float_as_uint(x), false, false);
  x = __uint_as_float(s[0]) + __uint_as_float(s[1]);
  auto t = __builtin_amdgcn_permlane32_swap(__float_as_uint(x), __float_as_uint(x), false, false);
  return __uint_as_float(t[0]) + __uint_as_float(t[1]);
}
__device__ __forceinline__ float row16_sum(float x) {
  x += dpp<XOR1>(x); x += dpp<XOR2>(x); x += dpp<XOR7>(x); x += dpp<XOR8>(x); return x;
}
__device__ __forceinline__ float wave_sum(float x) { return xrow16_sum(row16_sum(x)); }

constexpr size_t WS_QP = 0;
constexpr size_t WS_GP = WS_QP + 8 * 4096;
constexpr size_t WS_UP = WS_GP + 8 * F;
constexpr size_t WS_PO = WS_UP + 8 * F;
constexpr size_t WS_ML = WS_PO + (size_t)H * NSPLIT * DH;
constexpr size_t WS_OP = WS_ML + (size_t)H * NSPLIT * 2;

__device__ __forceinline__ void layernorm_to_lds(const __hip_bfloat16* x, float* xn, float* red) {
  const int tid = threadIdx.x;
  u32x2 v = *reinterpret_cast<const u32x2*>(x + 4 * tid);
  float a0 = bflo(v[0]), a1 = bfhi(v[0]), a2 = bflo(v[1]), a3 = bfhi(v[1]);
  float s = wave_sum(a0 + a1 + a2 + a3);
  if ((tid & 63) == 0) red[tid >> 6] = s;
  __syncthreads();
  float mean = (red[0] + red[1] + red[2] + red[3]) * (1.f / D);
  float d0 = a0 - mean, d1 = a1 - mean, d2 = a2 - mean, d3 = a3 - mean;
  float q = wave_sum(d0 * d0 + d1 * d1 + d2 * d2 + d3 * d3);
  if ((tid & 63) == 0) red[4 + (tid >> 6)] = q;
  __syncthreads();
  float var = (red[4] + red[5] + red[6] + red[7]) * (1.f / D);
  if (RND >= 2) {
    float mb = rbf(mean), vb = rbf(var);
    float sd = rbf(sqrtf(rbf(vb + 1e-5f)));
    d0 = rbf(rbf(a0 - mb) / sd); d1 = rbf(rbf(a1 - mb) / sd); d2 = rbf(rbf(a2 - mb) / sd); d3 = rbf(rbf(a3 - mb) / sd);
  } else {
    float rs = 1.f / sqrtf(var + 1e-5f);
    d0 *= rs; d1 *= rs; d2 *= rs; d3 *= rs;
    if (RND >= 1) { d0 = rbf(d0); d1 = rbf(d1); d2 = rbf(d2); d3 = rbf(d3); }
  }
  *reinterpret_cast<f32x4*>(xn + 4 * tid) = f32x4{d0, d1, d2, d3};
  __syncthreads();
}

__device__ __forceinline__ void tile_gemv(const __hip_bfloat16* W, int ld, int r0, int c0, const float* vec, float* red  , float* out) {
  const int tid = threadIdx.x, rg = tid >> 4, c = tid & 15;
  u32x4 w[8];
#pragma unroll
  for (int i = 0; i < 8; ++i) w[i] = __builtin_nontemporal_load(reinterpret_cast<const u32x4*>(W + (size_t)(r0 + rg + 16 * i) * ld + c0 + 8 * c));
  float acc[8] = {0, 0, 0, 0, 0, 0, 0, 0};
#pragma unroll
  for (int i = 0; i < 8; ++i) {
    float xv = vec[rg + 16 * i];
#pragma unroll
    for (int k = 0; k < 4; ++k) { acc[2 * k] = fmaf(xv, bflo(w[i][k]), acc[2 * k]); acc[2 * k + 1] = fmaf(xv, bfhi(w[i][k]), acc[2 * k + 1]); }
  }
#pragma unroll
  for (int k = 0; k < 8; ++k) acc[k] = xrow16_sum(acc[k]);
  const int wave = tid >> 6;
  if ((tid & 63) < 16) {
    *reinterpret_cast<f32x4*>(red + wave * 128 + 8 * c) = f32x4{acc[0], acc[1], acc[2], acc[3]};
    *reinterpret_cast<f32x4*>(red + wave * 128 + 8 * c + 4) = f32x4{acc[4], acc[5], acc[6], acc[7]};
  }
  __syncthreads();
  if (tid < 128) out[tid] = red[tid] + red[128 + tid] + red[256 + tid] + red[384 + tid];
}

__global__ void __launch_bounds__(256) k_pre(const __hip_bfloat16* x, const __hip_bfloat16* wq, const __hip_bfloat16* wg, const __hip_bfloat16* wu, float* ws) {
  __shared__ __attribute__((aligned(16))) float xn[D];
  __shared__ __attribute__((aligned(16))) float red[512];
  layernorm_to_lds(x, xn, red);
  int b = blockIdx.x;
  const int ks = b & 7; b >>= 3;
  if (b < 32) tile_gemv(wq, 4096, ks * 128, b * 128, xn + ks * 128, red, ws + WS_QP + ks * 4096 + b * 128);
  else if (b < 60) tile_gemv(wg, F, ks * 128, (b - 32) * 128, xn + ks * 128, red, ws + WS_GP + ks * F + (b - 32) * 128);
  else tile_gemv(wu, F, ks * 128, (b - 60) * 128, xn + ks * 128, red, ws + WS_UP + ks * F + (b - 60) * 128);
}

constexpr int U = 16;
__global__ void __launch_bounds__(256) k_attn(const __hip_bfloat16* kc, const __hip_bfloat16* vc, const __hip_bfloat16* wd, float* ws) {
  __shared__ __attribute__((aligned(16))) float lds[1024];
  const int tid = threadIdx.x, lane = tid & 63, wave = __builtin_amdgcn_readfirstlane(tid >> 6), r = lane >> 4, c = lane & 15;
  int b = blockIdx.x;
  if (b >= H * NSPLIT) {
    b -= H * NSPLIT;
    const int rb = b >> 3, cb = b & 7;
    if (tid < 128) {
      float g = 0, u = 0;
#pragma unroll
      for (int s = 0; s < 8; ++s) { g += ws[WS_GP + s * F + rb * 128 + tid]; u += ws[WS_UP + s * F + rb * 128 + tid]; }
      float hh;
      if (RND >= 2) { g = rbf(g); u = rbf(u); hh = rbf(rbf(1.f / (1.f + __expf(-g))) * u); }
      else hh = u / (1.f + __expf(-g));
      lds[512 + tid] = hh;
    }
    __syncthreads();
    tile_gemv(wd, D, rb * 128, cb * 128, lds + 512, lds, ws + WS_OP + (size_t)(32 + rb) * D + cb * 128);
    return;
  }
  const int h = b / NSPLIT, s = b % NSPLIT;
  constexpr int KEYS = SK / NSPLIT;
  constexpr int NB = KEYS / (4 * 4 * U);
  if (tid < 128) {
    float q = 0;
#pragma unroll
    for (int ss = 0; ss < 8; ++ss) q += ws[WS_QP + ss * 4096 + h * 128 + tid];
    lds[tid] = q;
  }
  __syncthreads();
  unsigned qh[4], ql[4];
#pragma unroll
  for (int k = 0; k < 4; ++k) {
    float a = lds[8 * c + 2 * k], bb = lds[8 * c + 2 * k + 1];
    qh[k] = cvtpk(a, bb);
    ql[k] = cvtpk(a - bflo(qh[k]), bb - bfhi(qh[k]));
  }
  __syncthreads();
  const size_t base = ((size_t)h * SK + (size_t)s * KEYS) * DH + (size_t)wave * (4 * U * DH) + r * DH + 8 * c;
  const u32x4* kp = reinterpret_cast<const u32x4*>(kc + base);
  const u32x4* vp = reinterpret_cast<const u32x4*>(vc + base);
  constexpr int TS = 4 * DH / 8;
  constexpr int BS = 4 * 4 * U * DH / 8;
  constexpr float SC = 0.088388347648318440f, L2E = 1.4426950408889634f;
  float m = -INFINITY, l = 0.f, acc[8] = {0, 0, 0, 0, 0, 0, 0, 0};
  u32x4 Kn[U], Vn[U];
#pragma unroll
  for (int t = 0; t < U; ++t) Kn[t] = __builtin_nontemporal_load(kp + t * TS);
#pragma unroll
  for (int t = 0; t < U; ++t) Vn[t] = __builtin_nontemporal_load(vp + t * TS);
  auto body = [&](int in, auto more_) {
    constexpr bool more = decltype(more_)::value;
    float x[U];
#pragma unroll
    for (int t = 0; t < U; ++t) {
      float d = 0.f;
#pragma unroll
      for (int k = 0; k < 4; ++k) d = dot2(Kn[t][k], qh[k], d);
      if (RND == 0) {
#pragma unroll
        for (int k = 0; k < 4; ++k) d = dot2(Kn[t][k], ql[k], d);
      }
      x[t] = d;
    }
    __builtin_amdgcn_sched_barrier(0);
    if constexpr (more) {
#pragma unroll
    for (int t = 0; t < U; ++t) Kn[t] = __builtin_nontemporal_load(kp + (size_t)in * BS + t * TS);
    }
    __builtin_amdgcn_sched_barrier(0);
#define TR_STEP(HALF, CTRL, BIT) _Pragma("unroll") for (int hh = 0; hh < (HALF); ++hh) { \
      const float lo_ = x[hh], hi_ = x[hh + (HALF)]; const float t1_ = lo_ + dpp<CTRL>(lo_), t2_ = hi_ + dpp<CTRL>(hi_); \
      x[hh] = (c & (BIT)) ? t2_ : t1_; }
    TR_STEP(8, XOR8, 8) TR_STEP(4, XOR7, 4) TR_STEP(2, XOR2, 2) TR_STEP(1, XOR1, 1)
#undef TR_STEP
    float sc;
    if (RND >= 2) sc = rbf(rbf(x[0]) * rbf(SC)) * L2E; else sc = x[0] * (SC * L2E);
    float pm = sc;
    pm = fmaxf(pm, dpp<XOR1>(pm)); pm = fmaxf(pm, dpp<XOR2>(pm)); pm = fmaxf(pm, dpp<XOR7>(pm)); pm = fmaxf(pm, dpp<XOR8>(pm));
    pm = xrow16_max(pm);
    const float mn = fmaxf(m, pm);
    const float alpha = __builtin_amdgcn_exp2f(m - mn);
    const float p = __builtin_amdgcn_exp2f(sc - mn);
    l = fmaf(l, alpha, p); m = mn;
#pragma unroll
    for (int k = 0; k < 8; ++k) acc[k] *= alpha;
#define PV(T) { const float pt = dpp<0x150 + (T)>(p); _Pragma("unroll") for (int k = 0; k < 4; ++k) { \
      acc[2 * k] = fmaf(pt, bflo(Vn[T][k]), acc[2 * k]); acc[2 * k + 1] = fmaf(pt, bfhi(Vn[T][k]), acc[2 * k + 1]); } }
    PV(0) PV(1) PV(2) PV(3) PV(4) PV(5) PV(6) PV(7) PV(8) PV(9) PV(10) PV(11) PV(12) PV(13) PV(14) PV(15)
#undef PV
    __builtin_amdgcn_sched_barrier(0);
    if constexpr (more) {
#pragma unroll
    for (int t = 0; t < U; ++t) Vn[t] = __builtin_nontemporal_load(vp + (size_t)in * BS + t * TS);
    }
    __builtin_amdgcn_sched_barrier(0);
  };
  for (int i = 1; i < NB; ++i) body(i, std::true_type{});
  body(0, std::false_type{});
#pragma unroll
  for (int k = 0; k < 8; ++k) acc[k] = xrow16_sum(acc[k]);
  l = wave_sum(l);
  float* rec = lds + wave * 132;
  if (r == 0) {
    *reinterpret_cast<f32x4*>(rec + 4 + 8 * c) = f32x4{acc[0], acc[1], acc[2], acc[3]};
    *reinterpret_cast<f32x4*>(rec + 4 + 8 * c + 4) = f32x4{acc[4], acc[5], acc[6], acc[7]};
    if (c == 0) { rec[0] = m; rec[1] = l; }
  }
  __syncthreads();
  if (tid < 128) {
    float M = fmaxf(fmaxf(lds[0], lds[132]), fmaxf(lds[264], lds[396]));
    float L = 0.f, o = 0.f;
#pragma unroll
    for (int w = 0; w < 4; ++w) { float wt = __builtin_amdgcn_exp2f(lds[w * 132] - M); L = fmaf(wt, lds[w * 132 + 1], L); o = fmaf(wt, lds[w * 132 + 4 + tid], o); }
    ws[WS_PO + (size_t)(h * NSPLIT + s) * DH + tid] = o;
    if (tid == 0) { ws[WS_ML + (h * NSPLIT + s) * 2] = M; ws[WS_ML + (h * NSPLIT + s) * 2 + 1] = L; }
  }
}

__global__ void __launch_bounds__(256) k_post(const __hip_bfloat16* wo, float* ws) {
  __shared__ __attribute__((aligned(16))) float lds[1024];
  const int tid = threadIdx.x, h = blockIdx.x >> 3, cb = blockIdx.x & 7;
  if (tid < 128) {
    float M = -INFINITY;
#pragma unroll
    for (int s = 0; s < NSPLIT; ++s) M = fmaxf(M, ws[WS_ML + (h * NSPLIT + s) * 2]);
    float L = 0.f, o = 0.f;
#pragma unroll
    for (int s = 0; s < NSPLIT; ++s) {
      float wt = __builtin_amdgcn_exp2f(ws[WS_ML + (h * NSPLIT + s) * 2] - M);
      L = fmaf(wt, ws[WS_ML + (h * NSPLIT + s) * 2 + 1], L);
      o = fmaf(wt, ws[WS_PO + (size_t)(h * NSPLIT + s) * DH + tid], o);
    }
    o = o / L;
    if (RND >= 2) o = rbf(o);
    lds[512 + tid] = o;
  }
  __syncthreads();
  tile_gemv(wo, D, h * 128, cb * 128, lds + 512, lds, ws + WS_OP + (size_t)h * D + cb * 128);
}

__global__ void __launch_bounds__(256) k_fin(const __hip_bfloat16* x, const float* ws, __hip_bfloat16* out) {
  const int j = blockIdx.x * 256 + threadIdx.x;
  float a = 0.f, f = 0.f;
#pragma unroll
  for (int i = 0; i < 32; ++i) a += ws[WS_OP + (size_t)i * D + j];
#pragma unroll
  for (int i = 32; i < 60; ++i) f += ws[WS_OP + (size_t)i * D + j];
  float xv = __bfloat162float(x[j]);
  float o;
  if (RND >= 2) o = rbf(xv + rbf(a)) + rbf(f); else o = xv + a + f;
  out[j] = __float2bfloat16(o);
}

extern "C" void kernel_launch(void* const* d_in, const int* in_sizes, int n_in, void* d_out, int out_size, void* d_ws, size_t ws_size, hipStream_t stream) {
  const __hip_bfloat16* x = (const __hip_bfloat16*)d_in[0];
  const __hip_bfloat16* kc = (const __hip_bfloat16*)d_in[1];
  const __hip_bfloat16* vc = (const __hip_bfloat16*)d_in[2];
  const __hip_bfloat16* wq = (const __hip_bfloat16*)d_in[3];
  const __hip_bfloat16* wo = (const __hip_bfloat16*)d_in[4];
  const __hip_bfloat16* wg = (const __hip_bfloat16*)d_in[6];
  const __hip_bfloat16* wu = (const __hip_bfloat16*)d_in[7];
  const __hip_bfloat16* wd = (const __hip_bfloat16*)d_in[8];
  float* ws = (float*)d_ws;
  k_pre<<<88 * 8, 256, 0, stream>>>(x, wq, wg, wu, ws);
  k_attn<<<H * NSPLIT + 28 * 8, 256, 0, stream>>>(kc, vc, wd, ws);
  k_post<<<256, 256, 0, stream>>>(wo, ws);
  k_fin<<<4, 256, 0, stream>>>(x, ws, (__hip_bfloat16*)d_out);
}
```

```cpp
#include <hip/hip_runtime.h>
#include <hip/hip_bf16.h>
#include <cstdint>
#include <type_traits>

#define DK 5
#define DV 6
#define W0 8
#define ROT 1
#define QSLEEP 10
#define TOUCH 1
#define PPRE 0

using bf16x2 = __attribute__((ext_vector_type(2))) __bf16;
using u32x4 = __attribute__((ext_vector_type(4))) unsigned;
using u32x2 = __attribute__((ext_vector_type(2))) unsigned;
using f32x4 = __attribute__((ext_vector_type(4))) float;
using f32x2 = __attribute__((ext_vector_type(2))) float;
typedef __attribute__((address_space(1))) unsigned long long gu64;
typedef __attribute__((address_space(1))) unsigned gu32;
typedef __amdgpu_buffer_rsrc_t rsrc_t;

constexpr int D = 1024, H = 32, SK = 65536, DH = 128, F = 3584, NFB = F / 128;
constexpr int NST = SK / 8 / 32 / 4;
constexpr unsigned SPIN_CAP = 1u << 17;
constexpr unsigned OOR = 0x80000000u;

constexpr unsigned WS_EPOCH = 0;
constexpr unsigned WS_QG = 16384;
constexpr unsigned WS_GU = WS_QG + H * 4096;
constexpr unsigned WS_HB = WS_GU + NFB * 8192;
constexpr unsigned WS_ML = WS_HB + NFB * 512;
constexpr unsigned WS_H1 = WS_ML + H * 128;
constexpr unsigned WS_PA = WS_H1 + H * 8192;
constexpr unsigned WS_END = WS_PA + 8 * H * 2048;

#define SB() __builtin_amdgcn_sched_barrier(0)
#define PIN(x) asm volatile("" : "+v"(x))
#define PIN8(a) asm volatile("" : "+v"(a[0]), "+v"(a[1]), "+v"(a[2]), "+v"(a[3]), "+v"(a[4]), "+v"(a[5]), "+v"(a[6]), "+v"(a[7]))
__device__ __forceinline__ void wg_barrier() { asm volatile("s_waitcnt lgkmcnt(0)\n\ts_barrier" ::: "memory"); }

__device__ __forceinline__ float dot2(unsigned a, unsigned b, float acc) {
  return __builtin_amdgcn_fdot2_f32_bf16(__builtin_bit_cast(bf16x2, a), __builtin_bit_cast(bf16x2, b), acc, false);
}
__device__ __forceinline__ unsigned cvtpk(float lo, float hi) {
  unsigned r; asm("v_cvt_pk_bf16_f32 %0, %1, %2" : "=v"(r) : "v"(lo), "v"(hi)); return r;
}
__device__ __forceinline__ float bflo(unsigned u) { return __builtin_bit_cast(float, u << 16); }
__device__ __forceinline__ float bfhi(unsigned u) { return __builtin_bit_cast(float, u & 0xffff0000u); }
__device__ __forceinline__ float rbf(float x) { return bflo(cvtpk(x, x)); }
__device__ __forceinline__ float u2f(unsigned u) { return __builtin_bit_cast(float, u); }
__device__ __forceinline__ unsigned f2u(float f) { return __builtin_bit_cast(unsigned, f); }
template <int CTRL> __device__ __forceinline__ float dpp(float x) {
  return __builtin_bit_cast(float, __builtin_amdgcn_mov_dpp(__builtin_bit_cast(int, x), CTRL, 0xf, 0xf, true));
}
constexpr int XOR1 = 0xB1, XOR2 = 0x4E, XOR7 = 0x141, XOR8 = 0x128, ROR4 = 0x124;
__device__ __forceinline__ float swap32_add(float a, float b) {
  auto t = __builtin_amdgcn_permlane32_swap(f2u(a), f2u(b), false, false); return u2f(t[0]) + u2f(t[1]);
}
__device__ __forceinline__ float swap16_add(float a, float b) {
  auto t = __builtin_amdgcn_permlane16_swap(f2u(a), f2u(b), false, false); return u2f(t[0]) + u2f(t[1]);
}
__device__ __forceinline__ float xrow_sum(float x) { x = swap16_add(x, x); return swap32_add(x, x); }
__device__ __forceinline__ float xrow_max(float x) {
  auto s = __builtin_amdgcn_permlane16_swap(f2u(x), f2u(x), false, false); x = fmaxf(u2f(s[0]), u2f(s[1]));
  auto t = __builtin_amdgcn_permlane32_swap(f2u(x), f2u(x), false, false); return fmaxf(u2f(t[0]), u2f(t[1]));
}
__device__ __forceinline__ float row_sum(float x) { x += dpp<XOR1>(x); x += dpp<XOR2>(x); x += dpp<XOR7>(x); x += dpp<XOR8>(x); return x; }
__device__ __forceinline__ float wave_sum(float x) { return xrow_sum(row_sum(x)); }
__device__ __forceinline__ f32x2 rows_reduce(const float (&a)[8]) {
  const float y0 = swap32_add(a[0], a[4]), y1 = swap32_add(a[1], a[5]), y2 = swap32_add(a[2], a[6]), y3 = swap32_add(a[3], a[7]);
  return f32x2{swap16_add(y0, y2), swap16_add(y1, y3)};
}
__device__ __forceinline__ float rows8_reduce(const float (&a)[8], int lane) {
  const f32x2 z = rows_reduce(a);
  const float t1 = z[0] + dpp<XOR8>(z[0]), t2 = z[1] + dpp<XOR8>(z[1]);
  return (lane & 8) ? t2 : t1;
}
__device__ __forceinline__ unsigned plo(unsigned w0, unsigned w1) { return __builtin_amdgcn_perm(w1, w0, 0x05040100u); }
__device__ __forceinline__ unsigned phi(unsigned w0, unsigned w1) { return __builtin_amdgcn_perm(w1, w0, 0x07060302u); }
__device__ __forceinline__ void mac_rows(float (&acc)[8], u32x4 w0, u32x4 w1, unsigned xp) {
#pragma unroll
  for (int k = 0; k < 4; ++k) { acc[2 * k] = dot2(plo(w0[k], w1[k]), xp, acc[2 * k]); acc[2 * k + 1] = dot2(phi(w0[k], w1[k]), xp, acc[2 * k + 1]); }
}
__device__ __forceinline__ rsrc_t mkrs(const void* p, unsigned bytes) { return __builtin_amdgcn_make_buffer_rsrc(const_cast<void*>(p), 0, bytes, 0x00020000); }
__device__ __forceinline__ u32x4 ldnt(rsrc_t rs, unsigned voff, unsigned soff) { return __builtin_amdgcn_raw_buffer_load_b128(rs, voff, soff, 2); }
__device__ __forceinline__ u32x4 gload(rsrc_t rs, unsigned voff, unsigned soff) { return __builtin_amdgcn_raw_buffer_load_b128(rs, voff, soff, 16); }
__device__ __forceinline__ void gstore(rsrc_t rs, unsigned voff, unsigned soff, float v0, float v1, unsigned tA, unsigned tB) {
  __builtin_amdgcn_raw_buffer_store_b128(u32x4{f2u(v0), tA, f2u(v1), tB}, rs, voff + soff, 0, 16);
}
struct LN { float mb, sd; };
__device__ __forceinline__ LN ln_stats(u32x4 a, u32x4 b) {
  float v[16];
#pragma unroll
  for (int k = 0; k < 4; ++k) { v[2 * k] = bflo(a[k]); v[2 * k + 1] = bfhi(a[k]); v[8 + 2 * k] = bflo(b[k]); v[9 + 2 * k] = bfhi(b[k]); }
  float s = 0.f;
#pragma unroll
  for (int k = 0; k < 16; ++k) s += v[k];
  const float mean = wave_sum(s) * (1.f / D);
  float q = 0.f;
#pragma unroll
  for (int k = 0; k < 16; ++k) { const float d = v[k] - mean; q = fmaf(d, d, q); }
  const float var = wave_sum(q) * (1.f / D);
  return LN{rbf(mean), rbf(sqrtf(rbf(rbf(var) + 1e-5f)))};
}
__device__ __forceinline__ unsigned ln_pair(unsigned u, LN p) { return cvtpk(rbf(bflo(u) - p.mb) / p.sd, rbf(bfhi(u) - p.mb) / p.sd); }

template <int V> using IC = std::integral_constant<int, V>;
template <int A, int B, class Fn> __device__ __forceinline__ void sfor(Fn&& f) { if constexpr (A < B) { f(IC<A>{}); sfor<A + 1, B>(f); } }

__global__ void __launch_bounds__(320, 2) fused_decode(const __hip_bfloat16* __restrict__ x, const __hip_bfloat16* __restrict__ kc, const __hip_bfloat16* __restrict__ vc,
                                                    const __hip_bfloat16* __restrict__ wq, const __hip_bfloat16* __restrict__ wo, const __hip_bfloat16* __restrict__ wg,
                                                    const __hip_bfloat16* __restrict__ wu, const __hip_bfloat16* __restrict__ wd, __hip_bfloat16* __restrict__ out, char* ws) {
  __shared__ __attribute__((aligned(16))) float scs[5][NST][64];
  __shared__ __attribute__((aligned(16))) float part[4][128];
  __shared__ __attribute__((aligned(16))) float partf[4][128];
  __shared__ __attribute__((aligned(16))) float ml[4][2];
  __shared__ __attribute__((aligned(16))) unsigned vecb[64];
  __shared__ __attribute__((aligned(16))) unsigned hb[64];
  const int tid = threadIdx.x, lane = tid & 63, wave = __builtin_amdgcn_readfirstlane(tid >> 6), r = lane >> 4, c = lane & 15;
  const int h = blockIdx.x >> 3, s = blockIdx.x & 7;
  const bool ffn = h < NFB;
  const unsigned lo16 = lane * 16;
  const size_t kvoff = (size_t)h * (SK * DH) + (size_t)s * (SK / 8 * DH);
#define STAMP(i)
#define SSTAMP(i)

  if (wave < 4) {
    u32x4 xa = reinterpret_cast<const u32x4*>(x)[2 * lane], xb = reinterpret_cast<const u32x4*>(x)[2 * lane + 1];
    const int row0 = 256 * (s >> 1) + 64 * wave + 8 * (lane >> 3), col0 = 128 * h + 64 * (s & 1) + 8 * (lane & 7);
    u32x4 xr = *reinterpret_cast<const u32x4*>(x + row0);
    SB();
    const rsrc_t rq = mkrs(wq, D * H * DH * 2), ro = mkrs(wo, D * H * DH * 2), rg = mkrs(wg, D * F * 2), ru = mkrs(wu, D * F * 2), rd = mkrs(wd, D * F * 2);
    const rsrc_t rk = mkrs(kc + kvoff, SK / 8 * DH * 2), rv = mkrs(vc + kvoff, SK / 8 * DH * 2);
    const unsigned vq = (row0 * (H * DH) + col0) * 2, vg = (row0 * F + col0) * 2, vo = ((128 * h + 32 * wave + 8 * r) * D + 128 * s + 8 * c) * 2;
#define KV(RS, I, P) ldnt(RS, lo16 + ((P) & 3) * 1024, (4 * (I) + (ROT ? wave ^ ((I) & 1) : wave)) * 8192 + ((P) >> 2) * 4096)
    u32x4 R[8];
    unsigned wol[16], woh[16], wdl[16], wdh[16];
    auto head = [&](auto ffn_) {
      constexpr bool FF = decltype(ffn_)::value;
      constexpr int NL = FF ? 40 : 16;
      u32x4 W[NL];
      auto issue = [&](auto n_) {
        constexpr int n = decltype(n_)::value, i = n & 7, m = n >> 3;
        if constexpr (n >= NL) { if constexpr (n - NL < DK) R[n - NL] = KV(rk, 0, n - NL); }
        else if constexpr (m == 0) W[n] = ldnt(rq, vq, i * (H * DH * 2));
        else if constexpr (!FF || m == 3) W[n] = ldnt(ro, vo, i * (D * 2));
        else if constexpr (m == 1) W[n] = ldnt(rg, vg, i * (F * 2));
        else if constexpr (m == 2) W[n] = ldnt(ru, vg, i * (F * 2));
        else W[n] = ldnt(rd, vo, i * (D * 2));
        SB();
      };
      sfor<0, W0>(issue);
      SSTAMP(25);
      PIN(xa); PIN(xb); PIN(xr);
      const LN ln = ln_stats(xa, xb);
#pragma unroll
      for (int j = 0; j < 4; ++j) xr[j] = ln_pair(xr[j], ln);
      PIN(xr); SSTAMP(16);
      float a[8];
      sfor<0, NL / 2>([&](auto j_) {
        constexpr int j = decltype(j_)::value, m = j >> 2, jj = j & 3;
        constexpr bool mac = m == 0 || (FF && m < 3);
        if constexpr (mac) {
          if constexpr (jj == 0) {
#pragma unroll
            for (int k = 0; k < 8; ++k) a[k] = 0.f;
          }
          mac_rows(a, W[2 * j], W[2 * j + 1], xr[jj]); PIN8(a);
        } else {
#pragma unroll
          for (int k = 0; k < 4; ++k) {
            unsigned lo = plo(W[2 * j][k], W[2 * j + 1][k]), hi = phi(W[2 * j][k], W[2 * j + 1][k]); PIN(lo); PIN(hi);
            if constexpr (m == 4) { wdl[4 * jj + k] = lo; wdh[4 * jj + k] = hi; } else { wol[4 * jj + k] = lo; woh[4 * jj + k] = hi; }
          }
        }
        SB();
        issue(IC<W0 + 2 * j>{}); issue(IC<W0 + 2 * j + 1>{});
        if constexpr (mac && jj == 3) {
          const float v = rows8_reduce(a, lane);
          const int col = 8 * (lane & 7) + (lane >> 3);
          if constexpr (m == 0) { part[wave][col] = v; SSTAMP(17); wg_barrier(); SSTAMP(20); }
          else partf[wave][64 * (m - 1) + col] = v;
          SB();
        }
      });
      sfor<W0 + NL, NL + DK>(issue);
      if constexpr (!FF) {
#pragma unroll
        for (int k = 0; k < 16; ++k) wdl[k] = wdh[k] = 0u;
      }
    };
    if (ffn) head(std::true_type{}); else head(std::false_type{});
    SSTAMP(18);
    wg_barrier();
    const u32x4 q = *reinterpret_cast<const u32x4*>(&vecb[4 * c]);
    SSTAMP(19);

    constexpr float L2E = 1.4426950408889634f;
    const float scb = rbf(0.088388347648318440f);
    float m = -INFINITY, l = 0.f;
    auto kstep = [&](int i, auto last_) {
      constexpr bool last = decltype(last_)::value;
      float xs[8];
#pragma unroll
      for (int t = 0; t < 8; ++t) {
        float d = dot2(R[t][0], q[0], 0.f); d = dot2(R[t][1], q[1], d); d = dot2(R[t][2], q[2], d); d = dot2(R[t][3], q[3], d);
        xs[t] = d; PIN(xs[t]); SB();
        const int n = t + DK;
        if (n < 8) R[n] = KV(rk, i, n); else if (!last) R[n - 8] = KV(rk, i + 1, n - 8); else R[n - 8] = KV(rv, 0, n - 8);
        SB();
      }
#define TR_STEP(HALF, CTRL, BIT) _Pragma("unroll") for (int hh = 0; hh < (HALF); ++hh) { \
      const float t1_ = xs[hh] + dpp<CTRL>(xs[hh]), t2_ = xs[hh + (HALF)] + dpp<CTRL>(xs[hh + (HALF)]); xs[hh] = (c & (BIT)) ? t2_ : t1_; }
      TR_STEP(4, XOR8, 8) TR_STEP(2, XOR7, 4) TR_STEP(1, XOR2, 2)
#undef TR_STEP
      const float dotf = xs[0] + dpp<XOR1>(xs[0]);
      const float sc = rbf(rbf(dotf) * scb) * L2E;
      m = fmaxf(m, sc);
      scs[wave][i][lane] = sc;
    };
    for (int i = 0; i < NST - 1; ++i) kstep(i, std::false_type{});
    kstep(NST - 1, std::true_type{});
#pragma unroll
    for (int p = DK; p < DV; ++p) { R[p] = KV(rv, 0, p); SB(); }
    SSTAMP(22);
    m = fmaxf(m, dpp<XOR2>(m)); m = fmaxf(m, dpp<XOR7>(m)); m = fmaxf(m, dpp<XOR8>(m));
    f32x2 acc[4] = {{0, 0}, {0, 0}, {0, 0}, {0, 0}};
    float pn = PPRE ? __builtin_amdgcn_exp2f(scs[wave][0][lane] - m) : 0.f;
    auto vstep = [&](int i, auto last_) {
      constexpr bool last = decltype(last_)::value;
      float sn;
      if (PPRE && !last) { sn = scs[wave][i + 1][lane]; SB(); }
      const float p_ = PPRE ? pn : __builtin_amdgcn_exp2f(scs[wave][i][lane] - m);
      l += p_;
#define PV(T) { const float pt = dpp<0x150 + 2 * (T)>(p_); const f32x2 p2 = {pt, pt}; \
      _Pragma("unroll") for (int k = 0; k < 4; ++k) acc[k] = __builtin_elementwise_fma(p2, f32x2{bflo(R[T][k]), bfhi(R[T][k])}, acc[k]); \
      PIN(acc[0]); PIN(acc[1]); PIN(acc[2]); PIN(acc[3]); SB(); \
      const int n = (T) + DV; if (n < 8) R[n & 7] = KV(rv, i, n); else if (!last) R[n & 7] = KV(rv, i + 1, n - 8); SB(); }
      PV(0) PV(1) PV(2) PV(3)
      if (PPRE && !last) { pn = __builtin_amdgcn_exp2f(sn - m); PIN(pn); SB(); }
      PV(4) PV(5) PV(6) PV(7)
#undef PV
    };
    for (int i = 0; i < NST - 1; ++i) vstep(i, std::false_type{});
    vstep(NST - 1, std::true_type{});
    {
      const float M = xrow_max(m), e = __builtin_amdgcn_exp2f(m - M);
      const float a[8] = {acc[0][0] * e, acc[0][1] * e, acc[1][0] * e, acc[1][1] * e, acc[2][0] * e, acc[2][1] * e, acc[3][0] * e, acc[3][1] * e};
      *reinterpret_cast<f32x2*>(&part[wave][8 * c + 2 * r]) = rows_reduce(a);
      const float L = wave_sum(l * e) * 0.5f;
      if (lane == 0) *reinterpret_cast<f32x2*>(&ml[wave][0]) = f32x2{M, L};
    }
    wg_barrier();
    auto tile = [&](const unsigned (&wl)[16], const unsigned (&wh)[16], const unsigned* v, float* dst) {
      const u32x4 ob = *reinterpret_cast<const u32x4*>(v + 16 * wave + 4 * r);
      float a[8] = {0, 0, 0, 0, 0, 0, 0, 0};
#pragma unroll
      for (int j = 0; j < 4; ++j)
#pragma unroll
        for (int k = 0; k < 4; ++k) { a[2 * k] = dot2(wl[4 * j + k], ob[j], a[2 * k]); a[2 * k + 1] = dot2(wh[4 * j + k], ob[j], a[2 * k + 1]); }
      *reinterpret_cast<f32x2*>(dst + 8 * c + 2 * r) = rows_reduce(a);
    };
    tile(wdl, wdh, hb, partf[wave]);
    wg_barrier();
    tile(wol, woh, vecb, part[wave]);
    wg_barrier();
    return;
  }

  __builtin_amdgcn_s_setprio(3);
  const rsrc_t rs = __builtin_amdgcn_make_buffer_rsrc(ws, 0, WS_END, 0x00020000);
  const unsigned long long ep = __hip_atomic_load((gu64*)(ws + WS_EPOCH), __ATOMIC_RELAXED, __HIP_MEMORY_SCOPE_AGENT);
  const unsigned short xo = reinterpret_cast<const unsigned short*>(x)[128 * s + 4 * h + (lane & 3)];
  unsigned touch;
  {
    const int hf = ffn ? h : 0, e = lane & 1;
    const size_t gofs = (size_t)(256 * (s >> 1) + 255 * e) * F + 128 * hf + 64 * (s & 1), oofs = (size_t)(128 * h + 127 * e) * D + 128 * s, dofs = (size_t)(128 * hf + 127 * e) * D + 128 * s;
    const size_t kofs = kvoff + (size_t)e * (SK / 8 * DH - 64);
    const int w = (lane >> 1) & 7;
    const __hip_bfloat16* p = w == 0 ? kc + kofs : w == 1 ? vc + kofs : w == 2 ? wg + gofs : w == 3 ? wu + gofs : w == 4 ? wo + oofs : wd + dofs;
    touch = *reinterpret_cast<const unsigned*>(p);
    SB();
  }
  const unsigned tA = __builtin_amdgcn_readfirstlane((unsigned)ep) + 1u, tB = __builtin_amdgcn_readfirstlane((unsigned)(ep >> 32)) + 1u;
  auto good = [&](u32x4 g) { return ((g[1] ^ tA) | (g[3] ^ tB)) == 0u; };
  auto sum4 = [&](const float (&p)[4][128], int i) { return (p[0][i] + p[1][i]) + (p[2][i] + p[3][i]); };
  auto sum4x2 = [&](int i) {
    const f32x2 a = *reinterpret_cast<const f32x2*>(&part[0][i]), b = *reinterpret_cast<const f32x2*>(&part[1][i]);
    const f32x2 cc = *reinterpret_cast<const f32x2*>(&part[2][i]), d = *reinterpret_cast<const f32x2*>(&part[3][i]);
    return (a + b) + (cc + d);
  };
  int npoll = 0;
  u32x4 P[8];
  auto poll = [&](auto n_, unsigned want, unsigned miss, unsigned voff, unsigned base, unsigned stride) {
    constexpr int n = decltype(n_)::value;
    u32x4 N[n];
#pragma unroll
    for (int p = 0; p < n; ++p) { N[p] = gload(rs, voff | ((want >> p & 1u) ? 0u : OOR), base + p * stride); SB(); }
#pragma unroll
    for (int p = 0; p < n; ++p) { P[p] = (want >> p & 1u) ? N[p] : P[p]; if (__all(good(N[p]))) miss &= ~(want & 1u << p); }
    return miss;
  };
#pragma unroll
  for (int p = 0; p < 8; ++p) P[p] = u32x4{0, 0, 0, 0};
  wg_barrier();
  STAMP(1);
  { const f32x2 v = sum4x2(2 * (lane & 31)); gstore(rs, lane < 32 ? lo16 : OOR, WS_QG + (h * 8 + s) * 512, v[0], v[1], tA, tB); }
  SB();
  __builtin_amdgcn_s_sleep(QSLEEP);
  {
    unsigned miss = 0xfu;
    for (unsigned spin = 0; miss != 0u && spin < SPIN_CAP; ++spin) {
      asm volatile("" ::: "memory");
      miss = poll(IC<4>{}, miss, miss, lo16, WS_QG + h * 4096, 1024);
      ++npoll;
    }
    vecb[lane] = cvtpk((u2f(P[0][0]) + u2f(P[1][0])) + (u2f(P[2][0]) + u2f(P[3][0])), (u2f(P[0][2]) + u2f(P[1][2])) + (u2f(P[2][2]) + u2f(P[3][2])));
  }
  asm volatile("" :: "v"(touch));
  STAMP(2);
  wg_barrier();
  __builtin_amdgcn_s_setprio(0);
  if (ffn) {
    gstore(rs, lo16, WS_GU + (h * 8 + s) * 1024, sum4(partf, lane), sum4(partf, 64 + lane), tA, tB);
    u32x4 g;
    for (unsigned spin = 0; spin < SPIN_CAP; ++spin) {
      __builtin_amdgcn_s_sleep(127);
      asm volatile("" ::: "memory");
      g = gload(rs, ((s >> 2) + 2 * r) * 1024 + (16 * (s & 3) + c) * 16, WS_GU + h * 8192);
      npoll += 256;
      if (__all(good(g))) break;
    }
    const float gs = rbf(xrow_sum(u2f(g[0]))), us = rbf(xrow_sum(u2f(g[2])));
    const float hv = rbf(1.f / (1.f + __expf(-gs))) * us;
    const unsigned w2 = cvtpk(hv, dpp<XOR1>(hv));
    gstore(rs, (lane & 0x33) == 0 ? (lane >> 2) * 16 : OOR, WS_HB + (h * 8 + s) * 64, u2f(w2), dpp<XOR2>(u2f(w2)), tA, tB);
    for (unsigned spin = 0; spin < SPIN_CAP; ++spin) {
      __builtin_amdgcn_s_sleep(127);
      asm volatile("" ::: "memory");
      g = gload(rs, (lane & 31) * 16, WS_HB + h * 512);
      npoll += 65536;
      if (__all(good(g))) break;
    }
    if (lane < 32) *reinterpret_cast<u32x2*>(&hb[2 * lane]) = u32x2{g[0], g[2]};
  } else {
    hb[lane] = 0u;
  }
  STAMP(3);
  __builtin_amdgcn_s_setprio(3);

  wg_barrier();
  {
    const f32x4 m01 = *reinterpret_cast<const f32x4*>(&ml[0][0]), m23 = *reinterpret_cast<const f32x4*>(&ml[2][0]);
    const float M = fmaxf(fmaxf(m01[0], m01[2]), fmaxf(m23[0], m23[2]));
    const float e0 = __builtin_amdgcn_exp2f(m01[0] - M), e1 = __builtin_amdgcn_exp2f(m01[2] - M), e2 = __builtin_amdgcn_exp2f(m23[0] - M), e3 = __builtin_amdgcn_exp2f(m23[2] - M);
    const f32x2 a = *reinterpret_cast<const f32x2*>(&part[0][2 * lane]), b = *reinterpret_cast<const f32x2*>(&part[1][2 * lane]);
    const f32x2 cc = *reinterpret_cast<const f32x2*>(&part[2][2 * lane]), d = *reinterpret_cast<const f32x2*>(&part[3][2 * lane]);
    const f32x2 o = (a * e0 + b * e1) + (cc * e2 + d * e3);
    const float L = (m01[1] * e0 + m01[3] * e1) + (m23[1] * e2 + m23[3] * e3);
    gstore(rs, lo16, WS_H1 + (h * 8 + s) * 1024, o[0], o[1], tA, tB);
    gstore(rs, lane == 0 ? 0u : OOR, WS_ML + (h * 8 + s) * 16, M, L, tA, tB);
  }
  STAMP(8);
  {
    u32x4 g;
    unsigned miss = 0xffu, want = 0xffu;
    for (unsigned spin = 0; spin < SPIN_CAP; ++spin) {
      asm volatile("" ::: "memory");
      g = gload(rs, (lane & 7) * 16, WS_ML + h * 128);
      SB();
      miss = poll(IC<8>{}, want, miss, lo16, WS_H1 + h * 8192, 1024);
      const unsigned flags = (unsigned)__ballot(good(g)) & 0xffu;
      if (miss == 0u && flags == 0xffu) break;
      want = __builtin_popcount(miss) <= 2 ? miss : miss & flags;
    }
    const float mp = u2f(g[0]);
    float M = fmaxf(mp, dpp<XOR1>(mp)); M = fmaxf(M, dpp<XOR2>(M)); M = fmaxf(M, dpp<XOR7>(M));
    const float e = __builtin_amdgcn_exp2f(mp - M);
    float L = e * u2f(g[2]); L += dpp<XOR1>(L); L += dpp<XOR2>(L); L += dpp<XOR7>(L);
    float o0 = 0.f, o1 = 0.f;
#pragma unroll
    for (int p = 0; p < 8; ++p) { const float ep_ = u2f(__builtin_amdgcn_readlane(f2u(e), p)); o0 = fmaf(ep_, u2f(P[p][0]), o0); o1 = fmaf(ep_, u2f(P[p][2]), o1); }
    vecb[lane] = cvtpk(o0 / L, o1 / L);
  }
  wg_barrier();
  STAMP(9);
  const float f0 = sum4(partf, lane), f1 = sum4(partf, 64 + lane);
  wg_barrier();
  gstore(rs, lo16, WS_PA + (s * H + h) * 2048, sum4(part, lane), f0, tA, tB);
  gstore(rs, lo16, WS_PA + (s * H + h) * 2048 + 1024, sum4(part, 64 + lane), f1, tA, tB);
  STAMP(10);
  {
    const unsigned coloff = (lane >> 2) * 2048 + (4 * h + (lane & 3)) * 16;
    unsigned miss = 3u;
    for (unsigned spin = 0; miss != 0u && spin < SPIN_CAP; ++spin) {
      asm volatile("" ::: "memory");
      miss = poll(IC<2>{}, miss, miss, coloff, WS_PA + s * H * 2048, 16 * 2048);
    }
    auto col_reduce = [&](float v) { v += dpp<XOR8>(v); v += dpp<ROR4>(v); return xrow_sum(v); };
    const float a = rbf(col_reduce(u2f(P[0][0]) + u2f(P[1][0]))), f = rbf(col_reduce(u2f(P[0][2]) + u2f(P[1][2])));
    const float res = rbf(bflo(xo) + a) + f;
    const unsigned w2 = cvtpk(res, dpp<XOR1>(res));
    const unsigned w3 = f2u(dpp<XOR2>(u2f(w2)));
    if (lane == 0) *reinterpret_cast<u32x2*>(out + 128 * s + 4 * h) = u32x2{w2, w3};
  }
  if (blockIdx.x == 0 && lane == 0) __hip_atomic_store((gu64*)(ws + WS_EPOCH), ((unsigned long long)tB << 32) | tA, __ATOMIC_RELAXED, __HIP_MEMORY_SCOPE_AGENT);
}

extern "C" void kernel_launch(void* const* d_in, const int* in_sizes, int n_in, void* d_out, int out_size, void* d_ws, size_t ws_size, hipStream_t stream) {
  fused_decode<<<256, 320, 0, stream>>>((const __hip_bfloat16*)d_in[0], (const __hip_bfloat16*)d_in[1], (const __hip_bfloat16*)d_in[2], (const __hip_bfloat16*)d_in[3],
                                        (const __hip_bfloat16*)d_in[4], (const __hip_bfloat16*)d_in[6], (const __hip_bfloat16*)d_in[7], (const __hip_bfloat16*)d_in[8],
                                        (__hip_bfloat16*)d_out, (char*)d_ws);
}
```

```cpp
#define DEADLINE 152
#define DLWG 100
#include <hip/hip_runtime.h>
#include <hip/hip_bf16.h>
#include <cstdint>
#include <type_traits>

#define DK 5
#define DV 6
#define W0 8
#define ROT 1
#define QSLEEP 10
#define TOUCH 1
#define PPRE 0

using bf16x2 = __attribute__((ext_vector_type(2))) __bf16;
using u32x4 = __attribute__((ext_vector_type(4))) unsigned;
using u32x2 = __attribute__((ext_vector_type(2))) unsigned;
using f32x4 = __attribute__((ext_vector_type(4))) float;
using f32x2 = __attribute__((ext_vector_type(2))) float;
typedef __attribute__((address_space(1))) unsigned long long gu64;
typedef __attribute__((address_space(1))) unsigned gu32;
typedef __amdgpu_buffer_rsrc_t rsrc_t;

constexpr int D = 1024, H = 32, SK = 65536, DH = 128, F = 3584, NFB = F / 128;
constexpr int NST = SK / 8 / 32 / 4;
constexpr unsigned SPIN_CAP = 1u << 17;
constexpr unsigned OOR = 0x80000000u;

constexpr unsigned WS_EPOCH = 0;
constexpr unsigned WS_QG = 16384;
constexpr unsigned WS_GU = WS_QG + H * 4096;
constexpr unsigned WS_HB = WS_GU + NFB * 8192;
constexpr unsigned WS_ML = WS_HB + NFB * 512;
constexpr unsigned WS_H1 = WS_ML + H * 128;
constexpr unsigned WS_PA = WS_H1 + H * 8192;
constexpr unsigned WS_END = WS_PA + 8 * H * 2048;

#define SB() __builtin_amdgcn_sched_barrier(0)
#define PIN(x) asm volatile("" : "+v"(x))
#define PIN8(a) asm volatile("" : "+v"(a[0]), "+v"(a[1]), "+v"(a[2]), "+v"(a[3]), "+v"(a[4]), "+v"(a[5]), "+v"(a[6]), "+v"(a[7]))
__device__ __forceinline__ void wg_barrier() { asm volatile("s_waitcnt lgkmcnt(0)\n\ts_barrier" ::: "memory"); }

__device__ __forceinline__ float dot2(unsigned a, unsigned b, float acc) {
  return __builtin_amdgcn_fdot2_f32_bf16(__builtin_bit_cast(bf16x2, a), __builtin_bit_cast(bf16x2, b), acc, false);
}
__device__ __forceinline__ unsigned cvtpk(float lo, float hi) {
  unsigned r; asm("v_cvt_pk_bf16_f32 %0, %1, %2" : "=v"(r) : "v"(lo), "v"(hi)); return r;
}
__device__ __forceinline__ float bflo(unsigned u) { return __builtin_bit_cast(float, u << 16); }
__device__ __forceinline__ float bfhi(unsigned u) { return __builtin_bit_cast(float, u & 0xffff0000u); }
__device__ __forceinline__ float rbf(float x) { return bflo(cvtpk(x, x)); }
__device__ __forceinline__ float u2f(unsigned u) { return __builtin_bit_cast(float, u); }
__device__ __forceinline__ unsigned f2u(float f) { return __builtin_bit_cast(unsigned, f); }
template <int CTRL> __device__ __forceinline__ float dpp(float x) {
  return __builtin_bit_cast(float, __builtin_amdgcn_mov_dpp(__builtin_bit_cast(int, x), CTRL, 0xf, 0xf, true));
}
constexpr int XOR1 = 0xB1, XOR2 = 0x4E, XOR7 = 0x141, XOR8 = 0x128, ROR4 = 0x124;
__device__ __forceinline__ float swap32_add(float a, float b) {
  auto t = __builtin_amdgcn_permlane32_swap(f2u(a), f2u(b), false, false); return u2f(t[0]) + u2f(t[1]);
}
__device__ __forceinline__ float swap16_add(float a, float b) {
  auto t = __builtin_amdgcn_permlane16_swap(f2u(a), f2u(b), false, false); return u2f(t[0]) + u2f(t[1]);
}
__device__ __forceinline__ float xrow_sum(float x) { x = swap16_add(x, x); return swap32_add(x, x); }
__device__ __forceinline__ float xrow_max(float x) {
  auto s = __builtin_amdgcn_permlane16_swap(f2u(x), f2u(x), false, false); x = fmaxf(u2f(s[0]), u2f(s[1]));
  auto t = __builtin_amdgcn_permlane32_swap(f2u(x), f2u(x), false, false); return fmaxf(u2f(t[0]), u2f(t[1]));
}
__device__ __forceinline__ float row_sum(float x) { x += dpp<XOR1>(x); x += dpp<XOR2>(x); x += dpp<XOR7>(x); x += dpp<XOR8>(x); return x; }
__device__ __forceinline__ float wave_sum(float x) { return xrow_sum(row_sum(x)); }
__device__ __forceinline__ f32x2 rows_reduce(const float (&a)[8]) {
  const float y0 = swap32_add(a[0], a[4]), y1 = swap32_add(a[1], a[5]), y2 = swap32_add(a[2], a[6]), y3 = swap32_add(a[3], a[7]);
  return f32x2{swap16_add(y0, y2), swap16_add(y1, y3)};
}
__device__ __forceinline__ float rows8_reduce(const float (&a)[8], int lane) {
  const f32x2 z = rows_reduce(a);
  const float t1 = z[0] + dpp<XOR8>(z[0]), t2 = z[1] + dpp<XOR8>(z[1]);
  return (lane & 8) ? t2 : t1;
}
__device__ __forceinline__ unsigned plo(unsigned w0, unsigned w1) { return __builtin_amdgcn_perm(w1, w0, 0x05040100u); }
__device__ __forceinline__ unsigned phi(unsigned w0, unsigned w1) { return __builtin_amdgcn_perm(w1, w0, 0x07060302u); }
__device__ __forceinline__ void mac_rows(float (&acc)[8], u32x4 w0, u32x4 w1, unsigned xp) {
#pragma unroll
  for (int k = 0; k < 4; ++k) { acc[2 * k] = dot2(plo(w0[k], w1[k]), xp, acc[2 * k]); acc[2 * k + 1] = dot2(phi(w0[k], w1[k]), xp, acc[2 * k + 1]); }
}
__device__ __forceinline__ rsrc_t mkrs(const void* p, unsigned bytes) { return __builtin_amdgcn_make_buffer_rsrc(const_cast<void*>(p), 0, bytes, 0x00020000); }
__device__ __forceinline__ u32x4 ldnt(rsrc_t rs, unsigned voff, unsigned soff) { return __builtin_amdgcn_raw_buffer_load_b128(rs, voff, soff, 2); }
__device__ __forceinline__ u32x4 gload(rsrc_t rs, unsigned voff, unsigned soff) { return __builtin_amdgcn_raw_buffer_load_b128(rs, voff, soff, 16); }
__device__ __forceinline__ void gstore(rsrc_t rs, unsigned voff, unsigned soff, float v0, float v1, unsigned tA, unsigned tB) {
  __builtin_amdgcn_raw_buffer_store_b128(u32x4{f2u(v0), tA, f2u(v1), tB}, rs, voff + soff, 0, 16);
}
struct LN { float mb, sd; };
__device__ __forceinline__ LN ln_stats(u32x4 a, u32x4 b) {
  float v[16];
#pragma unroll
  for (int k = 0; k < 4; ++k) { v[2 * k] = bflo(a[k]); v[2 * k + 1] = bfhi(a[k]); v[8 + 2 * k] = bflo(b[k]); v[9 + 2 * k] = bfhi(b[k]); }
  float s = 0.f;
#pragma unroll
  for (int k = 0; k < 16; ++k) s += v[k];
  const float mean = wave_sum(s) * (1.f / D);
  float q = 0.f;
#pragma unroll
  for (int k = 0; k < 16; ++k) { const float d = v[k] - mean; q = fmaf(d, d, q); }
  const float var = wave_sum(q) * (1.f / D);
  return LN{rbf(mean), rbf(sqrtf(rbf(rbf(var) + 1e-5f)))};
}
__device__ __forceinline__ unsigned ln_pair(unsigned u, LN p) { return cvtpk(rbf(bflo(u) - p.mb) / p.sd, rbf(bfhi(u) - p.mb) / p.sd); }

template <int V> using IC = std::integral_constant<int, V>;
template <int A, int B, class Fn> __device__ __forceinline__ void sfor(Fn&& f) { if constexpr (A < B) { f(IC<A>{}); sfor<A + 1, B>(f); } }

__global__ void __launch_bounds__(320, 2) fused_decode(const __hip_bfloat16* __restrict__ x, const __hip_bfloat16* __restrict__ kc, const __hip_bfloat16* __restrict__ vc,
                                                    const __hip_bfloat16* __restrict__ wq, const __hip_bfloat16* __restrict__ wo, const __hip_bfloat16* __restrict__ wg,
                                                    const __hip_bfloat16* __restrict__ wu, const __hip_bfloat16* __restrict__ wd, __hip_bfloat16* __restrict__ out, char* ws) {
  __shared__ __attribute__((aligned(16))) float scs[5][NST][64];
  __shared__ __attribute__((aligned(16))) float part[4][128];
  __shared__ __attribute__((aligned(16))) float partf[4][128];
  __shared__ __attribute__((aligned(16))) float ml[4][2];
  __shared__ __attribute__((aligned(16))) unsigned vecb[64];
  __shared__ __attribute__((aligned(16))) unsigned hb[64];
  const int tid = threadIdx.x, lane = tid & 63, wave = __builtin_amdgcn_readfirstlane(tid >> 6), r = lane >> 4, c = lane & 15;
  const int h = blockIdx.x >> 3, s = blockIdx.x & 7;
  const bool ffn = h < NFB;
  const unsigned long long t_dl = __builtin_amdgcn_s_memrealtime();
  const unsigned lo16 = lane * 16;
  const size_t kvoff = (size_t)h * (SK * DH) + (size_t)s * (SK / 8 * DH);
#define STAMP(i)
#define SSTAMP(i)

  if (wave < 4) {
    u32x4 xa = reinterpret_cast<const u32x4*>(x)[2 * lane], xb = reinterpret_cast<const u32x4*>(x)[2 * lane + 1];
    const int row0 = 256 * (s >> 1) + 64 * wave + 8 * (lane >> 3), col0 = 128 * h + 64 * (s & 1) + 8 * (lane & 7);
    u32x4 xr = *reinterpret_cast<const u32x4*>(x + row0);
    SB();
    const rsrc_t rq = mkrs(wq, D * H * DH * 2), ro = mkrs(wo, D * H * DH * 2), rg = mkrs(wg, D * F * 2), ru = mkrs(wu, D * F * 2), rd = mkrs(wd, D * F * 2);
    const rsrc_t rk = mkrs(kc + kvoff, SK / 8 * DH * 2), rv = mkrs(vc + kvoff, SK / 8 * DH * 2);
    const unsigned vq = (row0 * (H * DH) + col0) * 2, vg = (row0 * F + col0) * 2, vo = ((128 * h + 32 * wave + 8 * r) * D + 128 * s + 8 * c) * 2;
#define KV(RS, I, P) ldnt(RS, lo16 + ((P) & 3) * 1024, (4 * (I) + (ROT ? wave ^ ((I) & 1) : wave)) * 8192 + ((P) >> 2) * 4096)
    u32x4 R[8];
    unsigned wol[16], woh[16], wdl[16], wdh[16];
    auto head = [&](auto ffn_) {
      constexpr bool FF = decltype(ffn_)::value;
      constexpr int NL = FF ? 40 : 16;
      u32x4 W[NL];
      auto issue = [&](auto n_) {
        constexpr int n = decltype(n_)::value, i = n & 7, m = n >> 3;
        if constexpr (n >= NL) { if constexpr (n - NL < DK) R[n - NL] = KV(rk, 0, n - NL); }
        else if constexpr (m == 0) W[n] = ldnt(rq, vq, i * (H * DH * 2));
        else if constexpr (!FF || m == 3) W[n] = ldnt(ro, vo, i * (D * 2));
        else if constexpr (m == 1) W[n] = ldnt(rg, vg, i * (F * 2));
        else if constexpr (m == 2) W[n] = ldnt(ru, vg, i * (F * 2));
        else W[n] = ldnt(rd, vo, i * (D * 2));
        SB();
      };
      sfor<0, W0>(issue);
      SSTAMP(25);
      PIN(xa); PIN(xb); PIN(xr);
      const LN ln = ln_stats(xa, xb);
#pragma unroll
      for (int j = 0; j < 4; ++j) xr[j] = ln_pair(xr[j], ln);
      PIN(xr); SSTAMP(16);
      float a[8];
      sfor<0, NL / 2>([&](auto j_) {
        constexpr int j = decltype(j_)::value, m = j >> 2, jj = j & 3;
        constexpr bool mac = m == 0 || (FF && m < 3);
        if constexpr (mac) {
          if constexpr (jj == 0) {
#pragma unroll
            for (int k = 0; k < 8; ++k) a[k] = 0.f;
          }
          mac_rows(a, W[2 * j], W[2 * j + 1], xr[jj]); PIN8(a);
        } else {
#pragma unroll
          for (int k = 0; k < 4; ++k) {
            unsigned lo = plo(W[2 * j][k], W[2 * j + 1][k]), hi = phi(W[2 * j][k], W[2 * j + 1][k]); PIN(lo); PIN(hi);
            if constexpr (m == 4) { wdl[4 * jj + k] = lo; wdh[4 * jj + k] = hi; } else { wol[4 * jj + k] = lo; woh[4 * jj + k] = hi; }
          }
        }
        SB();
        issue(IC<W0 + 2 * j>{}); issue(IC<W0 + 2 * j + 1>{});
        if constexpr (mac && jj == 3) {
          const float v = rows8_reduce(a, lane);
          const int col = 8 * (lane & 7) + (lane >> 3);
          if constexpr (m == 0) { part[wave][col] = v; SSTAMP(17); wg_barrier(); SSTAMP(20); }
          else partf[wave][64 * (m - 1) + col] = v;
          SB();
        }
      });
      sfor<W0 + NL, NL + DK>(issue);
      if constexpr (!FF) {
#pragma unroll
        for (int k = 0; k < 16; ++k) wdl[k] = wdh[k] = 0u;
      }
    };
    if (ffn) head(std::true_type{}); else head(std::false_type{});
    SSTAMP(18);
    wg_barrier();
    const u32x4 q = *reinterpret_cast<const u32x4*>(&vecb[4 * c]);
    SSTAMP(19);

    constexpr float L2E = 1.4426950408889634f;
    const float scb = rbf(0.088388347648318440f);
    float m = -INFINITY, l = 0.f;
    auto kstep = [&](int i, auto last_) {
      constexpr bool last = decltype(last_)::value;
      float xs[8];
#pragma unroll
      for (int t = 0; t < 8; ++t) {
        float d = dot2(R[t][0], q[0], 0.f); d = dot2(R[t][1], q[1], d); d = dot2(R[t][2], q[2], d); d = dot2(R[t][3], q[3], d);
        xs[t] = d; PIN(xs[t]); SB();
        const int n = t + DK;
        if (n < 8) R[n] = KV(rk, i, n); else if (!last) R[n - 8] = KV(rk, i + 1, n - 8); else R[n - 8] = KV(rv, 0, n - 8);
        SB();
      }
#define TR_STEP(HALF, CTRL, BIT) _Pragma("unroll") for (int hh = 0; hh < (HALF); ++hh) { \
      const float t1_ = xs[hh] + dpp<CTRL>(xs[hh]), t2_ = xs[hh + (HALF)] + dpp<CTRL>(xs[hh + (HALF)]); xs[hh] = (c & (BIT)) ? t2_ : t1_; }
      TR_STEP(4, XOR8, 8) TR_STEP(2, XOR7, 4) TR_STEP(1, XOR2, 2)
#undef TR_STEP
      const float dotf = xs[0] + dpp<XOR1>(xs[0]);
      const float sc = rbf(rbf(dotf) * scb) * L2E;
      m = fmaxf(m, sc);
      scs[wave][i][lane] = sc;
    };
    for (int i = 0; i < NST - 1; ++i) kstep(i, std::false_type{});
    kstep(NST - 1, std::true_type{});
#pragma unroll
    for (int p = DK; p < DV; ++p) { R[p] = KV(rv, 0, p); SB(); }
    SSTAMP(22);
    m = fmaxf(m, dpp<XOR2>(m)); m = fmaxf(m, dpp<XOR7>(m)); m = fmaxf(m, dpp<XOR8>(m));
    f32x2 acc[4] = {{0, 0}, {0, 0}, {0, 0}, {0, 0}};
    float pn = PPRE ? __builtin_amdgcn_exp2f(scs[wave][0][lane] - m) : 0.f;
    auto vstep = [&](int i, auto last_) {
      constexpr bool last = decltype(last_)::value;
      float sn;
      if (PPRE && !last) { sn = scs[wave][i + 1][lane]; SB(); }
      const float p_ = PPRE ? pn : __builtin_amdgcn_exp2f(scs[wave][i][lane] - m);
      l += p_;
#define PV(T) { const float pt = dpp<0x150 + 2 * (T)>(p_); const f32x2 p2 = {pt, pt}; \
      _Pragma("unroll") for (int k = 0; k < 4; ++k) acc[k] = __builtin_elementwise_fma(p2, f32x2{bflo(R[T][k]), bfhi(R[T][k])}, acc[k]); \
      PIN(acc[0]); PIN(acc[1]); PIN(acc[2]); PIN(acc[3]); SB(); \
      const int n = (T) + DV; if (n < 8) R[n & 7] = KV(rv, i, n); else if (!last) R[n & 7] = KV(rv, i + 1, n - 8); SB(); }
      PV(0) PV(1) PV(2) PV(3)
      if (PPRE && !last) { pn = __builtin_amdgcn_exp2f(sn - m); PIN(pn); SB(); }
      PV(4) PV(5) PV(6) PV(7)
#undef PV
    };
    for (int i = 0; i < NST - 1; ++i) vstep(i, std::false_type{});
    vstep(NST - 1, std::true_type{});
    {
      const float M = xrow_max(m), e = __builtin_amdgcn_exp2f(m - M);
      const float a[8] = {acc[0][0] * e, acc[0][1] * e, acc[1][0] * e, acc[1][1] * e, acc[2][0] * e, acc[2][1] * e, acc[3][0] * e, acc[3][1] * e};
      *reinterpret_cast<f32x2*>(&part[wave][8 * c + 2 * r]) = rows_reduce(a);
      const float L = wave_sum(l * e) * 0.5f;
      if (lane == 0) *reinterpret_cast<f32x2*>(&ml[wave][0]) = f32x2{M, L};
    }
    wg_barrier();
    auto tile = [&](const unsigned (&wl)[16], const unsigned (&wh)[16], const unsigned* v, float* dst) {
      const u32x4 ob = *reinterpret_cast<const u32x4*>(v + 16 * wave + 4 * r);
      float a[8] = {0, 0, 0, 0, 0, 0, 0, 0};
#pragma unroll
      for (int j = 0; j < 4; ++j)
#pragma unroll
        for (int k = 0; k < 4; ++k) { a[2 * k] = dot2(wl[4 * j + k], ob[j], a[2 * k]); a[2 * k + 1] = dot2(wh[4 * j + k], ob[j], a[2 * k + 1]); }
      *reinterpret_cast<f32x2*>(dst + 8 * c + 2 * r) = rows_reduce(a);
    };
    tile(wdl, wdh, hb, partf[wave]);
    wg_barrier();
    tile(wol, woh, vecb, part[wave]);
    wg_barrier();
    return;
  }

  __builtin_amdgcn_s_setprio(3);
  const rsrc_t rs = __builtin_amdgcn_make_buffer_rsrc(ws, 0, WS_END, 0x00020000);
  const unsigned long long ep = __hip_atomic_load((gu64*)(ws + WS_EPOCH), __ATOMIC_RELAXED, __HIP_MEMORY_SCOPE_AGENT);
  const unsigned short xo = reinterpret_cast<const unsigned short*>(x)[128 * s + 4 * h + (lane & 3)];
  unsigned touch;
  {
    const int hf = ffn ? h : 0, e = lane & 1;
    const size_t gofs = (size_t)(256 * (s >> 1) + 255 * e) * F + 128 * hf + 64 * (s & 1), oofs = (size_t)(128 * h + 127 * e) * D + 128 * s, dofs = (size_t)(128 * hf + 127 * e) * D + 128 * s;
    const size_t kofs = kvoff + (size_t)e * (SK / 8 * DH - 64);
    const int w = (lane >> 1) & 7;
    const __hip_bfloat16* p = w == 0 ? kc + kofs : w == 1 ? vc + kofs : w == 2 ? wg + gofs : w == 3 ? wu + gofs : w == 4 ? wo + oofs : wd + dofs;
    touch = *reinterpret_cast<const unsigned*>(p);
    SB();
  }
  const unsigned tA = __builtin_amdgcn_readfirstlane((unsigned)ep) + 1u, tB = __builtin_amdgcn_readfirstlane((unsigned)(ep >> 32)) + 1u;
  auto good = [&](u32x4 g) { return ((g[1] ^ tA) | (g[3] ^ tB)) == 0u; };
  auto sum4 = [&](const float (&p)[4][128], int i) { return (p[0][i] + p[1][i]) + (p[2][i] + p[3][i]); };
  auto sum4x2 = [&](int i) {
    const f32x2 a = *reinterpret_cast<const f32x2*>(&part[0][i]), b = *reinterpret_cast<const f32x2*>(&part[1][i]);
    const f32x2 cc = *reinterpret_cast<const f32x2*>(&part[2][i]), d = *reinterpret_cast<const f32x2*>(&part[3][i]);
    return (a + b) + (cc + d);
  };
  int npoll = 0;
  u32x4 P[8];
  auto poll = [&](auto n_, unsigned want, unsigned miss, unsigned voff, unsigned base, unsigned stride) {
    constexpr int n = decltype(n_)::value;
    u32x4 N[n];
#pragma unroll
    for (int p = 0; p < n; ++p) { N[p] = gload(rs, voff | ((want >> p & 1u) ? 0u : OOR), base + p * stride); SB(); }
#pragma unroll
    for (int p = 0; p < n; ++p) { P[p] = (want >> p & 1u) ? N[p] : P[p]; if (__all(good(N[p]))) miss &= ~(want & 1u << p); }
    return miss;
  };
#pragma unroll
  for (int p = 0; p < 8; ++p) P[p] = u32x4{0, 0, 0, 0};
  wg_barrier();
  STAMP(1);
  { const f32x2 v = sum4x2(2 * (lane & 31)); gstore(rs, lane < 32 ? lo16 : OOR, WS_QG + (h * 8 + s) * 512, v[0], v[1], tA, tB); }
  SB();
  __builtin_amdgcn_s_sleep(QSLEEP);
  {
    unsigned miss = 0xfu;
    for (unsigned spin = 0; miss != 0u && spin < SPIN_CAP; ++spin) {
      asm volatile("" ::: "memory");
      miss = poll(IC<4>{}, miss, miss, lo16, WS_QG + h * 4096, 1024);
      ++npoll;
    }
    vecb[lane] = cvtpk((u2f(P[0][0]) + u2f(P[1][0])) + (u2f(P[2][0]) + u2f(P[3][0])), (u2f(P[0][2]) + u2f(P[1][2])) + (u2f(P[2][2]) + u2f(P[3][2])));
  }
  asm volatile("" :: "v"(touch));
  STAMP(2);
  wg_barrier();
  __builtin_amdgcn_s_setprio(0);
  if (ffn) {
    gstore(rs, lo16, WS_GU + (h * 8 + s) * 1024, sum4(partf, lane), sum4(partf, 64 + lane), tA, tB);
    u32x4 g;
    for (unsigned spin = 0; spin < SPIN_CAP; ++spin) {
      __builtin_amdgcn_s_sleep(127);
      asm volatile("" ::: "memory");
      g = gload(rs, ((s >> 2) + 2 * r) * 1024 + (16 * (s & 3) + c) * 16, WS_GU + h * 8192);
      npoll += 256;
      if (__all(good(g))) break;
    }
    const float gs = rbf(xrow_sum(u2f(g[0]))), us = rbf(xrow_sum(u2f(g[2])));
    const float hv = rbf(1.f / (1.f + __expf(-gs))) * us;
    const unsigned w2 = cvtpk(hv, dpp<XOR1>(hv));
    gstore(rs, (lane & 0x33) == 0 ? (lane >> 2) * 16 : OOR, WS_HB + (h * 8 + s) * 64, u2f(w2), dpp<XOR2>(u2f(w2)), tA, tB);
    for (unsigned spin = 0; spin < SPIN_CAP; ++spin) {
      __builtin_amdgcn_s_sleep(127);
      asm volatile("" ::: "memory");
      g = gload(rs, (lane & 31) * 16, WS_HB + h * 512);
      npoll += 65536;
      if (__all(good(g))) break;
    }
    if (lane < 32) *reinterpret_cast<u32x2*>(&hb[2 * lane]) = u32x2{g[0], g[2]};
  } else {
    hb[lane] = 0u;
  }
  STAMP(3);
  __builtin_amdgcn_s_setprio(3);

  wg_barrier();
  {
    const f32x4 m01 = *reinterpret_cast<const f32x4*>(&ml[0][0]), m23 = *reinterpret_cast<const f32x4*>(&ml[2][0]);
    const float M = fmaxf(fmaxf(m01[0], m01[2]), fmaxf(m23[0], m23[2]));
    const float e0 = __builtin_amdgcn_exp2f(m01[0] - M), e1 = __builtin_amdgcn_exp2f(m01[2] - M), e2 = __builtin_amdgcn_exp2f(m23[0] - M), e3 = __builtin_amdgcn_exp2f(m23[2] - M);
    const f32x2 a = *reinterpret_cast<const f32x2*>(&part[0][2 * lane]), b = *reinterpret_cast<const f32x2*>(&part[1][2 * lane]);
    const f32x2 cc = *reinterpret_cast<const f32x2*>(&part[2][2 * lane]), d = *reinterpret_cast<const f32x2*>(&part[3][2 * lane]);
    const f32x2 o = (a * e0 + b * e1) + (cc * e2 + d * e3);
    const float L = (m01[1] * e0 + m01[3] * e1) + (m23[1] * e2 + m23[3] * e3);
    gstore(rs, lo16, WS_H1 + (h * 8 + s) * 1024, o[0], o[1], tA, tB);
    gstore(rs, lane == 0 ? 0u : OOR, WS_ML + (h * 8 + s) * 16, M, L, tA, tB);
  }
  STAMP(8);
  {
    u32x4 g;
    unsigned miss = 0xffu, want = 0xffu;
    for (unsigned spin = 0; spin < SPIN_CAP; ++spin) {
      asm volatile("" ::: "memory");
      g = gload(rs, (lane & 7) * 16, WS_ML + h * 128);
      SB();
      miss = poll(IC<8>{}, want, miss, lo16, WS_H1 + h * 8192, 1024);
      const unsigned flags = (unsigned)__ballot(good(g)) & 0xffu;
      if (miss == 0u && flags == 0xffu) break;
      want = __builtin_popcount(miss) <= 2 ? miss : miss & flags;
    }
    const float mp = u2f(g[0]);
    float M = fmaxf(mp, dpp<XOR1>(mp)); M = fmaxf(M, dpp<XOR2>(M)); M = fmaxf(M, dpp<XOR7>(M));
    const float e = __builtin_amdgcn_exp2f(mp - M);
    float L = e * u2f(g[2]); L += dpp<XOR1>(L); L += dpp<XOR2>(L); L += dpp<XOR7>(L);
    float o0 = 0.f, o1 = 0.f;
#pragma unroll
    for (int p = 0; p < 8; ++p) { const float ep_ = u2f(__builtin_amdgcn_readlane(f2u(e), p)); o0 = fmaf(ep_, u2f(P[p][0]), o0); o1 = fmaf(ep_, u2f(P[p][2]), o1); }
    vecb[lane] = cvtpk(o0 / L, o1 / L);
  }
  wg_barrier();
  STAMP(9);
  const float f0 = sum4(partf, lane), f1 = sum4(partf, 64 + lane);
  wg_barrier();
  gstore(rs, lo16, WS_PA + (s * H + h) * 2048, sum4(part, lane), f0, tA, tB);
  gstore(rs, lo16, WS_PA + (s * H + h) * 2048 + 1024, sum4(part, 64 + lane), f1, tA, tB);
  STAMP(10);
  {
    const unsigned coloff = (lane >> 2) * 2048 + (4 * h + (lane & 3)) * 16;
    unsigned miss = 3u;
    for (unsigned spin = 0; miss != 0u && spin < SPIN_CAP; ++spin) {
      asm volatile("" ::: "memory");
      miss = poll(IC<2>{}, miss, miss, coloff, WS_PA + s * H * 2048, 16 * 2048);
    }
    auto col_reduce = [&](float v) { v += dpp<XOR8>(v); v += dpp<ROR4>(v); return xrow_sum(v); };
    const float a = rbf(col_reduce(u2f(P[0][0]) + u2f(P[1][0]))), f = rbf(col_reduce(u2f(P[0][2]) + u2f(P[1][2])));
    const float res = rbf(bflo(xo) + a) + f;
    const unsigned w2 = cvtpk(res, dpp<XOR1>(res));
    const unsigned w3 = f2u(dpp<XOR2>(u2f(w2)));
    if (lane == 0) *reinterpret_cast<u32x2*>(out + 128 * s + 4 * h) = u32x2{w2, w3};
  }
  if (blockIdx.x == DLWG) while (__builtin_amdgcn_s_memrealtime() - t_dl < DEADLINE * 100ull) {}
  if (blockIdx.x == 0 && lane == 0) __hip_atomic_store((gu64*)(ws + WS_EPOCH), ((unsigned long long)tB << 32) | tA, __ATOMIC_RELAXED, __HIP_MEMORY_SCOPE_AGENT);
}

extern "C" void kernel_launch(void* const* d_in, const int* in_sizes, int n_in, void* d_out, int out_size, void* d_ws, size_t ws_size, hipStream_t stream) {
  fused_decode<<<256, 320, 0, stream>>>((const __hip_bfloat16*)d_in[0], (const __hip_bfloat16*)d_in[1], (const __hip_bfloat16*)d_in[2], (const __hip_bfloat16*)d_in[3],
                                        (const __hip_bfloat16*)d_in[4], (const __hip_bfloat16*)d_in[6], (const __hip_bfloat16*)d_in[7], (const __hip_bfloat16*)d_in[8],
                                        (__hip_bfloat16*)d_out, (char*)d_ws);
}
```

```cpp
#define DEADLINE 175
#define DLWG 100
#define OUTWT 1
#include <hip/hip_runtime.h>
#include <hip/hip_bf16.h>
#include <cstdint>
#include <type_traits>

#define DK 5
#define DV 6
#define W0 8
#define ROT 1
#define QSLEEP 10
#define TOUCH 1
#define NSCS 5
#define PPRE 0

using bf16x2 = __attribute__((ext_vector_type(2))) __bf16;
using u32x4 = __attribute__((ext_vector_type(4))) unsigned;
using u32x2 = __attribute__((ext_vector_type(2))) unsigned;
using f32x4 = __attribute__((ext_vector_type(4))) float;
using f32x2 = __attribute__((ext_vector_type(2))) float;
typedef __attribute__((address_space(1))) unsigned long long gu64;
typedef __attribute__((address_space(1))) unsigned gu32;
typedef __amdgpu_buffer_rsrc_t rsrc_t;

constexpr int D = 1024, H = 32, SK = 65536, DH = 128, F = 3584, NFB = F / 128;
constexpr int NST = SK / 8 / 32 / 4;
constexpr unsigned SPIN_CAP = 1u << 17;
constexpr unsigned OOR = 0x80000000u;

constexpr unsigned WS_EPOCH = 0;
constexpr unsigned WS_QG = 16384;
constexpr unsigned WS_GU = WS_QG + H * 4096;
constexpr unsigned WS_HB = WS_GU + NFB * 8192;
constexpr unsigned WS_ML = WS_HB + NFB * 512;
constexpr unsigned WS_H1 = WS_ML + H * 128;
constexpr unsigned WS_PA = WS_H1 + H * 8192;
constexpr unsigned WS_END = WS_PA + 8 * H * 2048;

#define SB() __builtin_amdgcn_sched_barrier(0)
#define PIN(x) asm volatile("" : "+v"(x))
#define PIN8(a) asm volatile("" : "+v"(a[0]), "+v"(a[1]), "+v"(a[2]), "+v"(a[3]), "+v"(a[4]), "+v"(a[5]), "+v"(a[6]), "+v"(a[7]))
__device__ __forceinline__ void wg_barrier() { asm volatile("s_waitcnt lgkmcnt(0)\n\ts_barrier" ::: "memory"); }

__device__ __forceinline__ float dot2(unsigned a, unsigned b, float acc) {
  return __builtin_amdgcn_fdot2_f32_bf16(__builtin_bit_cast(bf16x2, a), __builtin_bit_cast(bf16x2, b), acc, false);
}
__device__ __forceinline__ unsigned cvtpk(float lo, float hi) {
  unsigned r; asm("v_cvt_pk_bf16_f32 %0, %1, %2" : "=v"(r) : "v"(lo), "v"(hi)); return r;
}
__device__ __forceinline__ float bflo(unsigned u) { return __builtin_bit_cast(float, u << 16); }
__device__ __forceinline__ float bfhi(unsigned u) { return __builtin_bit_cast(float, u & 0xffff0000u); }
__device__ __forceinline__ float rbf(float x) { return bflo(cvtpk(x, x)); }
__device__ __forceinline__ float u2f(unsigned u) { return __builtin_bit_cast(float, u); }
__device__ __forceinline__ unsigned f2u(float f) { return __builtin_bit_cast(unsigned, f); }
template <int CTRL> __device__ __forceinline__ float dpp(float x) {
  return __builtin_bit_cast(float, __builtin_amdgcn_mov_dpp(__builtin_bit_cast(int, x), CTRL, 0xf, 0xf, true));
}
constexpr int XOR1 = 0xB1, XOR2 = 0x4E, XOR7 = 0x141, XOR8 = 0x128, ROR4 = 0x124;
__device__ __forceinline__ float swap32_add(float a, float b) {
  auto t = __builtin_amdgcn_permlane32_swap(f2u(a), f2u(b), false, false); return u2f(t[0]) + u2f(t[1]);
}
__device__ __forceinline__ float swap16_add(float a, float b) {
  auto t = __builtin_amdgcn_permlane16_swap(f2u(a), f2u(b), false, false); return u2f(t[0]) + u2f(t[1]);
}
__device__ __forceinline__ float xrow_sum(float x) { x = swap16_add(x, x); return swap32_add(x, x); }
__device__ __forceinline__ float xrow_max(float x) {
  auto s = __builtin_amdgcn_permlane16_swap(f2u(x), f2u(x), false, false); x = fmaxf(u2f(s[0]), u2f(s[1]));
  auto t = __builtin_amdgcn_permlane32_swap(f2u(x), f2u(x), false, false); return fmaxf(u2f(t[0]), u2f(t[1]));
}
__device__ __forceinline__ float row_sum(float x) { x += dpp<XOR1>(x); x += dpp<XOR2>(x); x += dpp<XOR7>(x); x += dpp<XOR8>(x); return x; }
__device__ __forceinline__ float wave_sum(float x) { return xrow_sum(row_sum(x)); }
__device__ __forceinline__ f32x2 rows_reduce(const float (&a)[8]) {
  const float y0 = swap32_add(a[0], a[4]), y1 = swap32_add(a[1], a[5]), y2 = swap32_add(a[2], a[6]), y3 = swap32_add(a[3], a[7]);
  return f32x2{swap16_add(y0, y2), swap16_add(y1, y3)};
}
__device__ __forceinline__ float rows8_reduce(const float (&a)[8], int lane) {
  const f32x2 z = rows_reduce(a);
  const float t1 = z[0] + dpp<XOR8>(z[0]), t2 = z[1] + dpp<XOR8>(z[1]);
  return (lane & 8) ? t2 : t1;
}
__device__ __forceinline__ unsigned plo(unsigned w0, unsigned w1) { return __builtin_amdgcn_perm(w1, w0, 0x05040100u); }
__device__ __forceinline__ unsigned phi(unsigned w0, unsigned w1) { return __builtin_amdgcn_perm(w1, w0, 0x07060302u); }
__device__ __forceinline__ void mac_rows(float (&acc)[8], u32x4 w0, u32x4 w1, unsigned xp) {
#pragma unroll
  for (int k = 0; k < 4; ++k) { acc[2 * k] = dot2(plo(w0[k], w1[k]), xp, acc[2 * k]); acc[2 * k + 1] = dot2(phi(w0[k], w1[k]), xp, acc[2 * k + 1]); }
}
__device__ __forceinline__ rsrc_t mkrs(const void* p, unsigned bytes) { return __builtin_amdgcn_make_buffer_rsrc(const_cast<void*>(p), 0, bytes, 0x00020000); }
__device__ __forceinline__ u32x4 ldnt(rsrc_t rs, unsigned voff, unsigned soff) { return __builtin_amdgcn_raw_buffer_load_b128(rs, voff, soff, 2); }
__device__ __forceinline__ u32x4 gload(rsrc_t rs, unsigned voff, unsigned soff) { return __builtin_amdgcn_raw_buffer_load_b128(rs, voff, soff, 16); }
__device__ __forceinline__ void gstore(rsrc_t rs, unsigned voff, unsigned soff, float v0, float v1, unsigned tA, unsigned tB) {
  __builtin_amdgcn_raw_buffer_store_b128(u32x4{f2u(v0), tA, f2u(v1), tB}, rs, voff + soff, 0, 16);
}
struct LN { float mb, sd; };
__device__ __forceinline__ LN ln_stats(u32x4 a, u32x4 b) {
  float v[16];
#pragma unroll
  for (int k = 0; k < 4; ++k) { v[2 * k] = bflo(a[k]); v[2 * k + 1] = bfhi(a[k]); v[8 + 2 * k] = bflo(b[k]); v[9 + 2 * k] = bfhi(b[k]); }
  float s = 0.f;
#pragma unroll
  for (int k = 0; k < 16; ++k) s += v[k];
  const float mean = wave_sum(s) * (1.f / D);
  float q = 0.f;
#pragma unroll
  for (int k = 0; k < 16; ++k) { const float d = v[k] - mean; q = fmaf(d, d, q); }
  const float var = wave_sum(q) * (1.f / D);
  return LN{rbf(mean), rbf(sqrtf(rbf(rbf(var) + 1e-5f)))};
}
__device__ __forceinline__ unsigned ln_pair(unsigned u, LN p) { return cvtpk(rbf(bflo(u) - p.mb) / p.sd, rbf(bfhi(u) - p.mb) / p.sd); }

template <int V> using IC = std::integral_constant<int, V>;
template <int A, int B, class Fn> __device__ __forceinline__ void sfor(Fn&& f) { if constexpr (A < B) { f(IC<A>{}); sfor<A + 1, B>(f); } }

__global__ void __launch_bounds__(320, 2) fused_decode(const __hip_bfloat16* __restrict__ x, const __hip_bfloat16* __restrict__ kc, const __hip_bfloat16* __restrict__ vc,
                                                    const __hip_bfloat16* __restrict__ wq, const __hip_bfloat16* __restrict__ wo, const __hip_bfloat16* __restrict__ wg,
                                                    const __hip_bfloat16* __restrict__ wu, const __hip_bfloat16* __restrict__ wd, __hip_bfloat16* __restrict__ out, char* ws) {
  __shared__ __attribute__((aligned(16))) float scs[NSCS][NST][64];
  __shared__ __attribute__((aligned(16))) float part[4][128];
  __shared__ __attribute__((aligned(16))) float partf[4][128];
  __shared__ __attribute__((aligned(16))) float ml[4][2];
  __shared__ __attribute__((aligned(16))) unsigned vecb[64];
  __shared__ __attribute__((aligned(16))) unsigned hb[64];
  const int tid = threadIdx.x, lane = tid & 63, wave = __builtin_amdgcn_readfirstlane(tid >> 6), r = lane >> 4, c = lane & 15;
  const int h = blockIdx.x >> 3, s = blockIdx.x & 7;
  const bool ffn = h < NFB;
  const unsigned long long t_dl = __builtin_amdgcn_s_memrealtime();
  const unsigned lo16 = lane * 16;
  const size_t kvoff = (size_t)h * (SK * DH) + (size_t)s * (SK / 8 * DH);
#define STAMP(i)
#define SSTAMP(i)

  if (wave < 4) {
    u32x4 xa = reinterpret_cast<const u32x4*>(x)[2 * lane], xb = reinterpret_cast<const u32x4*>(x)[2 * lane + 1];
    const int row0 = 256 * (s >> 1) + 64 * wave + 8 * (lane >> 3), col0 = 128 * h + 64 * (s & 1) + 8 * (lane & 7);
    u32x4 xr = *reinterpret_cast<const u32x4*>(x + row0);
    SB();
    const rsrc_t rq = mkrs(wq, D * H * DH * 2), ro = mkrs(wo, D * H * DH * 2), rg = mkrs(wg, D * F * 2), ru = mkrs(wu, D * F * 2), rd = mkrs(wd, D * F * 2);
    const rsrc_t rk = mkrs(kc + kvoff, SK / 8 * DH * 2), rv = mkrs(vc + kvoff, SK / 8 * DH * 2);
    const unsigned vq = (row0 * (H * DH) + col0) * 2, vg = (row0 * F + col0) * 2, vo = ((128 * h + 32 * wave + 8 * r) * D + 128 * s + 8 * c) * 2;
#define KV(RS, I, P) ldnt(RS, lo16 + ((P) & 3) * 1024, (4 * (I) + (ROT ? wave ^ ((I) & 1) : wave)) * 8192 + ((P) >> 2) * 4096)
    u32x4 R[8];
    unsigned wol[16], woh[16], wdl[16], wdh[16];
    auto head = [&](auto ffn_) {
      constexpr bool FF = decltype(ffn_)::value;
      constexpr int NL = FF ? 40 : 16;
      u32x4 W[NL];
      auto issue = [&](auto n_) {
        constexpr int n = decltype(n_)::value, i = n & 7, m = n >> 3;
        if constexpr (n >= NL) { if constexpr (n - NL < DK) R[n - NL] = KV(rk, 0, n - NL); }
        else if constexpr (m == 0) W[n] = ldnt(rq, vq, i * (H * DH * 2));
        else if constexpr (!FF || m == 3) W[n] = ldnt(ro, vo, i * (D * 2));
        else if constexpr (m == 1) W[n] = ldnt(rg, vg, i * (F * 2));
        else if constexpr (m == 2) W[n] = ldnt(ru, vg, i * (F * 2));
        else W[n] = ldnt(rd, vo, i * (D * 2));
        SB();
      };
      sfor<0, W0>(issue);
      SSTAMP(25);
      PIN(xa); PIN(xb); PIN(xr);
      const LN ln = ln_stats(xa, xb);
#pragma unroll
      for (int j = 0; j < 4; ++j) xr[j] = ln_pair(xr[j], ln);
      PIN(xr); SSTAMP(16);
      float a[8];
      sfor<0, NL / 2>([&](auto j_) {
        constexpr int j = decltype(j_)::value, m = j >> 2, jj = j & 3;
        constexpr bool mac = m == 0 || (FF && m < 3);
        if constexpr (mac) {
          if constexpr (jj == 0) {
#pragma unroll
            for (int k = 0; k < 8; ++k) a[k] = 0.f;
          }
          mac_rows(a, W[2 * j], W[2 * j + 1], xr[jj]); PIN8(a);
        } else {
#pragma unroll
          for (int k = 0; k < 4; ++k) {
            unsigned lo = plo(W[2 * j][k], W[2 * j + 1][k]), hi = phi(W[2 * j][k], W[2 * j + 1][k]); PIN(lo); PIN(hi);
            if constexpr (m == 4) { wdl[4 * jj + k] = lo; wdh[4 * jj + k] = hi; } else { wol[4 * jj + k] = lo; woh[4 * jj + k] = hi; }
          }
        }
        SB();
        issue(IC<W0 + 2 * j>{}); issue(IC<W0 + 2 * j + 1>{});
        if constexpr (mac && jj == 3) {
          const float v = rows8_reduce(a, lane);
          const int col = 8 * (lane & 7) + (lane >> 3);
          if constexpr (m == 0) { part[wave][col] = v; SSTAMP(17); wg_barrier(); SSTAMP(20); }
          else partf[wave][64 * (m - 1) + col] = v;
          SB();
        }
      });
      sfor<W0 + NL, NL + DK>(issue);
      if constexpr (!FF) {
#pragma unroll
        for (int k = 0; k < 16; ++k) wdl[k] = wdh[k] = 0u;
      }
    };
    if (ffn) head(std::true_type{}); else head(std::false_type{});
    SSTAMP(18);
    wg_barrier();
    const u32x4 q = *reinterpret_cast<const u32x4*>(&vecb[4 * c]);
    SSTAMP(19);

    constexpr float L2E = 1.4426950408889634f;
    const float scb = rbf(0.088388347648318440f);
    float m = -INFINITY, l = 0.f;
    auto kstep = [&](int i, auto last_) {
      constexpr bool last = decltype(last_)::value;
      float xs[8];
#pragma unroll
      for (int t = 0; t < 8; ++t) {
        float d = dot2(R[t][0], q[0], 0.f); d = dot2(R[t][1], q[1], d); d = dot2(R[t][2], q[2], d); d = dot2(R[t][3], q[3], d);
        xs[t] = d; PIN(xs[t]); SB();
        const int n = t + DK;
        if (n < 8) R[n] = KV(rk, i, n); else if (!last) R[n - 8] = KV(rk, i + 1, n - 8); else R[n - 8] = KV(rv, 0, n - 8);
        SB();
      }
#define TR_STEP(HALF, CTRL, BIT) _Pragma("unroll") for (int hh = 0; hh < (HALF); ++hh) { \
      const float t1_ = xs[hh] + dpp<CTRL>(xs[hh]), t2_ = xs[hh + (HALF)] + dpp<CTRL>(xs[hh + (HALF)]); xs[hh] = (c & (BIT)) ? t2_ : t1_; }
      TR_STEP(4, XOR8, 8) TR_STEP(2, XOR7, 4) TR_STEP(1, XOR2, 2)
#undef TR_STEP
      const float dotf = xs[0] + dpp<XOR1>(xs[0]);
      const float sc = rbf(rbf(dotf) * scb) * L2E;
      m = fmaxf(m, sc);
      scs[wave][i][lane] = sc;
    };
    for (int i = 0; i < NST - 1; ++i) kstep(i, std::false_type{});
    kstep(NST - 1, std::true_type{});
#pragma unroll
    for (int p = DK; p < DV; ++p) { R[p] = KV(rv, 0, p); SB(); }
    SSTAMP(22);
    m = fmaxf(m, dpp<XOR2>(m)); m = fmaxf(m, dpp<XOR7>(m)); m = fmaxf(m, dpp<XOR8>(m));
    f32x2 acc[4] = {{0, 0}, {0, 0}, {0, 0}, {0, 0}};
    float pn = PPRE ? __builtin_amdgcn_exp2f(scs[wave][0][lane] - m) : 0.f;
    auto vstep = [&](int i, auto last_) {
      constexpr bool last = decltype(last_)::value;
      float sn;
      if (PPRE && !last) { sn = scs[wave][i + 1][lane]; SB(); }
      const float p_ = PPRE ? pn : __builtin_amdgcn_exp2f(scs[wave][i][lane] - m);
      l += p_;
#define PV(T) { const float pt = dpp<0x150 + 2 * (T)>(p_); const f32x2 p2 = {pt, pt}; \
      _Pragma("unroll") for (int k = 0; k < 4; ++k) acc[k] = __builtin_elementwise_fma(p2, f32x2{bflo(R[T][k]), bfhi(R[T][k])}, acc[k]); \
      PIN(acc[0]); PIN(acc[1]); PIN(acc[2]); PIN(acc[3]); SB(); \
      const int n = (T) + DV; if (n < 8) R[n & 7] = KV(rv, i, n); else if (!last) R[n & 7] = KV(rv, i + 1, n - 8); SB(); }
      PV(0) PV(1) PV(2) PV(3)
      if (PPRE && !last) { pn = __builtin_amdgcn_exp2f(sn - m); PIN(pn); SB(); }
      PV(4) PV(5) PV(6) PV(7)
#undef PV
    };
    for (int i = 0; i < NST - 1; ++i) vstep(i, std::false_type{});
    vstep(NST - 1, std::true_type{});
    {
      const float M = xrow_max(m), e = __builtin_amdgcn_exp2f(m - M);
      const float a[8] = {acc[0][0] * e, acc[0][1] * e, acc[1][0] * e, acc[1][1] * e, acc[2][0] * e, acc[2][1] * e, acc[3][0] * e, acc[3][1] * e};
      *reinterpret_cast<f32x2*>(&part[wave][8 * c + 2 * r]) = rows_reduce(a);
      const float L = wave_sum(l * e) * 0.5f;
      if (lane == 0) *reinterpret_cast<f32x2*>(&ml[wave][0]) = f32x2{M, L};
    }
    wg_barrier();
    auto tile = [&](const unsigned (&wl)[16], const unsigned (&wh)[16], const unsigned* v, float* dst) {
      const u32x4 ob = *reinterpret_cast<const u32x4*>(v + 16 * wave + 4 * r);
      float a[8] = {0, 0, 0, 0, 0, 0, 0, 0};
#pragma unroll
      for (int j = 0; j < 4; ++j)
#pragma unroll
        for (int k = 0; k < 4; ++k) { a[2 * k] = dot2(wl[4 * j + k], ob[j], a[2 * k]); a[2 * k + 1] = dot2(wh[4 * j + k], ob[j], a[2 * k + 1]); }
      *reinterpret_cast<f32x2*>(dst + 8 * c + 2 * r) = rows_reduce(a);
    };
    tile(wdl, wdh, hb, partf[wave]);
    wg_barrier();
    tile(wol, woh, vecb, part[wave]);
    wg_barrier();
    return;
  }

  __builtin_amdgcn_s_setprio(3);
  const rsrc_t rs = __builtin_amdgcn_make_buffer_rsrc(ws, 0, WS_END, 0x00020000);
  const unsigned long long ep = __hip_atomic_load((gu64*)(ws + WS_EPOCH), __ATOMIC_RELAXED, __HIP_MEMORY_SCOPE_AGENT);
  const unsigned short xo = reinterpret_cast<const unsigned short*>(x)[128 * s + 4 * h + (lane & 3)];
  unsigned touch;
  {
    const int hf = ffn ? h : 0, e = lane & 1;
    const size_t gofs = (size_t)(256 * (s >> 1) + 255 * e) * F + 128 * hf + 64 * (s & 1), oofs = (size_t)(128 * h + 127 * e) * D + 128 * s, dofs = (size_t)(128 * hf + 127 * e) * D + 128 * s;
    const size_t kofs = kvoff + (size_t)e * (SK / 8 * DH - 64);
    const int w = (lane >> 1) & 7;
    const __hip_bfloat16* p = w == 0 ? kc + kofs : w == 1 ? vc + kofs : w == 2 ? wg + gofs : w == 3 ? wu + gofs : w == 4 ? wo + oofs : wd + dofs;
    touch = *reinterpret_cast<const unsigned*>(p);
    SB();
  }
  const unsigned tA = __builtin_amdgcn_readfirstlane((unsigned)ep) + 1u, tB = __builtin_amdgcn_readfirstlane((unsigned)(ep >> 32)) + 1u;
  auto good = [&](u32x4 g) { return ((g[1] ^ tA) | (g[3] ^ tB)) == 0u; };
  auto sum4 = [&](const float (&p)[4][128], int i) { return (p[0][i] + p[1][i]) + (p[2][i] + p[3][i]); };
  auto sum4x2 = [&](int i) {
    const f32x2 a = *reinterpret_cast<const f32x2*>(&part[0][i]), b = *reinterpret_cast<const f32x2*>(&part[1][i]);
    const f32x2 cc = *reinterpret_cast<const f32x2*>(&part[2][i]), d = *reinterpret_cast<const f32x2*>(&part[3][i]);
    return (a + b) + (cc + d);
  };
  int npoll = 0;
  u32x4 P[8];
  auto poll = [&](auto n_, unsigned want, unsigned miss, unsigned voff, unsigned base, unsigned stride) {
    constexpr int n = decltype(n_)::value;
    u32x4 N[n];
#pragma unroll
    for (int p = 0; p < n; ++p) { N[p] = gload(rs, voff | ((want >> p & 1u) ? 0u : OOR), base + p * stride); SB(); }
#pragma unroll
    for (int p = 0; p < n; ++p) { P[p] = (want >> p & 1u) ? N[p] : P[p]; if (__all(good(N[p]))) miss &= ~(want & 1u << p); }
    return miss;
  };
#pragma unroll
  for (int p = 0; p < 8; ++p) P[p] = u32x4{0, 0, 0, 0};
  wg_barrier();
  STAMP(1);
  { const f32x2 v = sum4x2(2 * (lane & 31)); gstore(rs, lane < 32 ? lo16 : OOR, WS_QG + (h * 8 + s) * 512, v[0], v[1], tA, tB); }
  SB();
  __builtin_amdgcn_s_sleep(QSLEEP);
  {
    unsigned miss = 0xfu;
    for (unsigned spin = 0; miss != 0u && spin < SPIN_CAP; ++spin) {
      asm volatile("" ::: "memory");
      miss = poll(IC<4>{}, miss, miss, lo16, WS_QG + h * 4096, 1024);
      ++npoll;
    }
    vecb[lane] = cvtpk((u2f(P[0][0]) + u2f(P[1][0])) + (u2f(P[2][0]) + u2f(P[3][0])), (u2f(P[0][2]) + u2f(P[1][2])) + (u2f(P[2][2]) + u2f(P[3][2])));
  }
  asm volatile("" :: "v"(touch));
  STAMP(2);
  wg_barrier();
  __builtin_amdgcn_s_setprio(0);
  if (ffn) {
    gstore(rs, lo16, WS_GU + (h * 8 + s) * 1024, sum4(partf, lane), sum4(partf, 64 + lane), tA, tB);
    u32x4 g;
    for (unsigned spin = 0; spin < SPIN_CAP; ++spin) {
      __builtin_amdgcn_s_sleep(127);
      asm volatile("" ::: "memory");
      g = gload(rs, ((s >> 2) + 2 * r) * 1024 + (16 * (s & 3) + c) * 16, WS_GU + h * 8192);
      npoll += 256;
      if (__all(good(g))) break;
    }
    const float gs = rbf(xrow_sum(u2f(g[0]))), us = rbf(xrow_sum(u2f(g[2])));
    const float hv = rbf(1.f / (1.f + __expf(-gs))) * us;
    const unsigned w2 = cvtpk(hv, dpp<XOR1>(hv));
    gstore(rs, (lane & 0x33) == 0 ? (lane >> 2) * 16 : OOR, WS_HB + (h * 8 + s) * 64, u2f(w2), dpp<XOR2>(u2f(w2)), tA, tB);
    for (unsigned spin = 0; spin < SPIN_CAP; ++spin) {
      __builtin_amdgcn_s_sleep(127);
      asm volatile("" ::: "memory");
      g = gload(rs, (lane & 31) * 16, WS_HB + h * 512);
      npoll += 65536;
      if (__all(good(g))) break;
    }
    if (lane < 32) *reinterpret_cast<u32x2*>(&hb[2 * lane]) = u32x2{g[0], g[2]};
  } else {
    hb[lane] = 0u;
  }
  STAMP(3);
  __builtin_amdgcn_s_setprio(3);

  wg_barrier();
  {
    const f32x4 m01 = *reinterpret_cast<const f32x4*>(&ml[0][0]), m23 = *reinterpret_cast<const f32x4*>(&ml[2][0]);
    const float M = fmaxf(fmaxf(m01[0], m01[2]), fmaxf(m23[0], m23[2]));
    const float e0 = __builtin_amdgcn_exp2f(m01[0] - M), e1 = __builtin_amdgcn_exp2f(m01[2] - M), e2 = __builtin_amdgcn_exp2f(m23[0] - M), e3 = __builtin_amdgcn_exp2f(m23[2] - M);
    const f32x2 a = *reinterpret_cast<const f32x2*>(&part[0][2 * lane]), b = *reinterpret_cast<const f32x2*>(&part[1][2 * lane]);
    const f32x2 cc = *reinterpret_cast<const f32x2*>(&part[2][2 * lane]), d = *reinterpret_cast<const f32x2*>(&part[3][2 * lane]);
    const f32x2 o = (a * e0 + b * e1) + (cc * e2 + d * e3);
    const float L = (m01[1] * e0 + m01[3] * e1) + (m23[1] * e2 + m23[3] * e3);
    gstore(rs, lo16, WS_H1 + (h * 8 + s) * 1024, o[0], o[1], tA, tB);
    gstore(rs, lane == 0 ? 0u : OOR, WS_ML + (h * 8 + s) * 16, M, L, tA, tB);
  }
  STAMP(8);
  {
    u32x4 g;
    unsigned miss = 0xffu, want = 0xffu;
    for (unsigned spin = 0; spin < SPIN_CAP; ++spin) {
      asm volatile("" ::: "memory");
      g = gload(rs, (lane & 7) * 16, WS_ML + h * 128);
      SB();
      miss = poll(IC<8>{}, want, miss, lo16, WS_H1 + h * 8192, 1024);
      const unsigned flags = (unsigned)__ballot(good(g)) & 0xffu;
      if (miss == 0u && flags == 0xffu) break;
      want = __builtin_popcount(miss) <= 2 ? miss : miss & flags;
    }
    const float mp = u2f(g[0]);
    float M = fmaxf(mp, dpp<XOR1>(mp)); M = fmaxf(M, dpp<XOR2>(M)); M = fmaxf(M, dpp<XOR7>(M));
    const float e = __builtin_amdgcn_exp2f(mp - M);
    float L = e * u2f(g[2]); L += dpp<XOR1>(L); L += dpp<XOR2>(L); L += dpp<XOR7>(L);
    float o0 = 0.f, o1 = 0.f;
#pragma unroll
    for (int p = 0; p < 8; ++p) { const float ep_ = u2f(__builtin_amdgcn_readlane(f2u(e), p)); o0 = fmaf(ep_, u2f(P[p][0]), o0); o1 = fmaf(ep_, u2f(P[p][2]), o1); }
    vecb[lane] = cvtpk(o0 / L, o1 / L);
  }
  wg_barrier();
  STAMP(9);
  const float f0 = sum4(partf, lane), f1 = sum4(partf, 64 + lane);
  wg_barrier();
  gstore(rs, lo16, WS_PA + (s * H + h) * 2048, sum4(part, lane), f0, tA, tB);
  gstore(rs, lo16, WS_PA + (s * H + h) * 2048 + 1024, sum4(part, 64 + lane), f1, tA, tB);
  STAMP(10);
  {
    const unsigned coloff = (lane >> 2) * 2048 + (4 * h + (lane & 3)) * 16;
    unsigned miss = 3u;
    for (unsigned spin = 0; miss != 0u && spin < SPIN_CAP; ++spin) {
      asm volatile("" ::: "memory");
      miss = poll(IC<2>{}, miss, miss, coloff, WS_PA + s * H * 2048, 16 * 2048);
    }
    auto col_reduce = [&](float v) { v += dpp<XOR8>(v); v += dpp<ROR4>(v); return xrow_sum(v); };
    const float a = rbf(col_reduce(u2f(P[0][0]) + u2f(P[1][0]))), f = rbf(col_reduce(u2f(P[0][2]) + u2f(P[1][2])));
    const float res = rbf(bflo(xo) + a) + f;
    const unsigned w2 = cvtpk(res, dpp<XOR1>(res));
    const unsigned w3 = f2u(dpp<XOR2>(u2f(w2)));
    if (lane == 0) __hip_atomic_store((gu64*)(out + 128 * s + 4 * h), ((unsigned long long)w3 << 32) | w2, __ATOMIC_RELAXED, __HIP_MEMORY_SCOPE_SYSTEM);
  }
  if (blockIdx.x == DLWG) while (__builtin_amdgcn_s_memrealtime() - t_dl < DEADLINE * 100ull) {}
  if (blockIdx.x == 0 && lane == 0) __hip_atomic_store((gu64*)(ws + WS_EPOCH), ((unsigned long long)tB << 32) | tA, __ATOMIC_RELAXED, __HIP_MEMORY_SCOPE_AGENT);
}

extern "C" void kernel_launch(void* const* d_in, const int* in_sizes, int n_in, void* d_out, int out_size, void* d_ws, size_t ws_size, hipStream_t stream) {
  fused_decode<<<256, 320, 0, stream>>>((const __hip_bfloat16*)d_in[0], (const __hip_bfloat16*)d_in[1], (const __hip_bfloat16*)d_in[2], (const __hip_bfloat16*)d_in[3],
                                        (const __hip_bfloat16*)d_in[4], (const __hip_bfloat16*)d_in[6], (const __hip_bfloat16*)d_in[7], (const __hip_bfloat16*)d_in[8],
                                        (__hip_bfloat16*)d_out, (char*)d_ws);
}
```

```cpp
#include <hip/hip_runtime.h>
#include <hip/hip_bf16.h>
#include <cstdint>
#include <cstdio>
#include <type_traits>

using bf16x2 = __attribute__((ext_vector_type(2))) __bf16;
using u32x4 = __attribute__((ext_vector_type(4))) unsigned;
using u32x2 = __attribute__((ext_vector_type(2))) unsigned;
using f32x4 = __attribute__((ext_vector_type(4))) float;
using f32x2 = __attribute__((ext_vector_type(2))) float;
typedef __attribute__((address_space(1))) unsigned long long gu64;
typedef __amdgpu_buffer_rsrc_t rsrc_t;

constexpr int D = 1024, H = 32, SK = 65536, DH = 128, F = 3584, NFB = F / 128;
constexpr int NST = SK / 8 / 32 / 4;
constexpr unsigned SPIN_CAP = 1u << 17;
constexpr unsigned OOR = 0x80000000u;

constexpr int DHI = 7, DMID = 5, DLO = 4, W0 = 6, TWW = 5;
constexpr int TN = 111, TH = 40, BIAS = 30, RFIRST = 1, RLAST = 120, RPER = 8, RDLY = 2;
constexpr int KG = 2, KM = (NST - RDLY - RFIRST + RPER - 1) / RPER;

constexpr unsigned WS_EPOCH = 0;
constexpr unsigned WS_O = 16384;
constexpr unsigned WS_P = WS_O + 256 * 1024;
constexpr unsigned WS_LD = WS_P + 256 * 1024;
constexpr unsigned LDH = 512, LDK = 8 * LDH;
constexpr unsigned WS_GU = WS_LD + 16 * LDK;
constexpr unsigned WS_Q = WS_GU + 256 * 1024;
constexpr unsigned WS_HB = WS_Q + 128 * 1024;
constexpr unsigned WS_ML = WS_HB + 16384;
constexpr unsigned WS_END = WS_ML + 4096;

#define SB() __builtin_amdgcn_sched_barrier(0)
#define PIN(x) asm volatile("" : "+v"(x))
#define PIN8(a) asm volatile("" : "+v"(a[0]), "+v"(a[1]), "+v"(a[2]), "+v"(a[3]), "+v"(a[4]), "+v"(a[5]), "+v"(a[6]), "+v"(a[7]))
__device__ __forceinline__ void wg_barrier() { asm volatile("s_waitcnt lgkmcnt(0)\n\ts_barrier" ::: "memory"); }

__device__ __forceinline__ float dot2(unsigned a, unsigned b, float acc) {
  return __builtin_amdgcn_fdot2_f32_bf16(__builtin_bit_cast(bf16x2, a), __builtin_bit_cast(bf16x2, b), acc, false);
}
__device__ __forceinline__ unsigned cvtpk(float lo, float hi) { return __builtin_bit_cast(unsigned, __builtin_convertvector(f32x2{lo, hi}, bf16x2)); }
__device__ __forceinline__ float bflo(unsigned u) { return __builtin_bit_cast(float, u << 16); }
__device__ __forceinline__ float bfhi(unsigned u) { return __builtin_bit_cast(float, u & 0xffff0000u); }
__device__ __forceinline__ float rbf(float x) { return bflo(cvtpk(x, x)); }
__device__ __forceinline__ float u2f(unsigned u) { return __builtin_bit_cast(float, u); }
__device__ __forceinline__ unsigned f2u(float f) { return __builtin_bit_cast(unsigned, f); }
template <int CTRL> __device__ __forceinline__ float dpp(float x) {
  return __builtin_bit_cast(float, __builtin_amdgcn_mov_dpp(__builtin_bit_cast(int, x), CTRL, 0xf, 0xf, true));
}
constexpr int XOR1 = 0xB1, XOR2 = 0x4E, XOR7 = 0x141, XOR8 = 0x128, ROR4 = 0x124;
__device__ __forceinline__ float swap32_add(float a, float b) {
  auto t = __builtin_amdgcn_permlane32_swap(f2u(a), f2u(b), false, false); return u2f(t[0]) + u2f(t[1]);
}
__device__ __forceinline__ float swap16_add(float a, float b) {
  auto t = __builtin_amdgcn_permlane16_swap(f2u(a), f2u(b), false, false); return u2f(t[0]) + u2f(t[1]);
}
__device__ __forceinline__ float xrow_sum(float x) { x = swap16_add(x, x); return swap32_add(x, x); }
__device__ __forceinline__ float xrow_max(float x) {
  auto s = __builtin_amdgcn_permlane16_swap(f2u(x), f2u(x), false, false); x = fmaxf(u2f(s[0]), u2f(s[1]));
  auto t = __builtin_amdgcn_permlane32_swap(f2u(x), f2u(x), false, false); return fmaxf(u2f(t[0]), u2f(t[1]));
}
__device__ __forceinline__ float row_sum(float x) { x += dpp<XOR1>(x); x += dpp<XOR2>(x); x += dpp<XOR7>(x); x += dpp<XOR8>(x); return x; }
__device__ __forceinline__ float wave_sum(float x) { return xrow_sum(row_sum(x)); }
__device__ __forceinline__ f32x2 rows_reduce(const float (&a)[8]) {
  const float y0 = swap32_add(a[0], a[4]), y1 = swap32_add(a[1], a[5]), y2 = swap32_add(a[2], a[6]), y3 = swap32_add(a[3], a[7]);
  return f32x2{swap16_add(y0, y2), swap16_add(y1, y3)};
}
__device__ __forceinline__ float rows8_reduce(const float (&a)[8], int lane) {
  const f32x2 z = rows_reduce(a);
  const float t1 = z[0] + dpp<XOR8>(z[0]), t2 = z[1] + dpp<XOR8>(z[1]);
  return (lane & 8) ? t2 : t1;
}
__device__ __forceinline__ unsigned plo(unsigned w0, unsigned w1) { return __builtin_amdgcn_perm(w1, w0, 0x05040100u); }
__device__ __forceinline__ unsigned phi(unsigned w0, unsigned w1) { return __builtin_amdgcn_perm(w1, w0, 0x07060302u); }
__device__ __forceinline__ void mac_rows(float (&acc)[8], u32x4 w0, u32x4 w1, unsigned xp) {
#pragma unroll
  for (int k = 0; k < 4; ++k) { acc[2 * k] = dot2(plo(w0[k], w1[k]), xp, acc[2 * k]); acc[2 * k + 1] = dot2(phi(w0[k], w1[k]), xp, acc[2 * k + 1]); }
}
__device__ __forceinline__ rsrc_t mkrs(const void* p, unsigned bytes) { return __builtin_amdgcn_make_buffer_rsrc(const_cast<void*>(p), 0, bytes, 0x00020000); }
__device__ __forceinline__ u32x4 ldnt(rsrc_t rs, unsigned voff, unsigned soff) { return __builtin_amdgcn_raw_buffer_load_b128(rs, voff, soff, 2); }
__device__ __forceinline__ u32x4 gload(rsrc_t rs, unsigned voff, unsigned soff) { return __builtin_amdgcn_raw_buffer_load_b128(rs, voff, soff, 16); }
__device__ __forceinline__ void gstore(rsrc_t rs, unsigned voff, unsigned soff, float v0, float v1, unsigned tA, unsigned tB) {
  __builtin_amdgcn_raw_buffer_store_b128(u32x4{f2u(v0), tA, f2u(v1), tB}, rs, voff + soff, 0, 16);
}
struct LN { float mb, rs; };
__device__ __forceinline__ LN ln_stats(u32x4 a, u32x4 b) {
  float s = 0.f;
#pragma unroll
  for (int k = 0; k < 4; ++k) { s = dot2(a[k], 0x3f803f80u, s); s = dot2(b[k], 0x3f803f80u, s); }
  const float mean = wave_sum(s) * (1.f / D);
  f32x2 q = {0.f, 0.f};
#pragma unroll
  for (int k = 0; k < 4; ++k) {
    const f32x2 da = f32x2{bflo(a[k]), bfhi(a[k])} - mean, db = f32x2{bflo(b[k]), bfhi(b[k])} - mean;
    q = __builtin_elementwise_fma(da, da, q); q = __builtin_elementwise_fma(db, db, q);
  }
  const float var = wave_sum(q[0] + q[1]) * (1.f / D);
  return LN{rbf(mean), __builtin_amdgcn_rcpf(rbf(__builtin_amdgcn_sqrtf(rbf(rbf(var) + 1e-5f))))};
}
__device__ __forceinline__ unsigned ln_pair(unsigned u, LN p) {
  const unsigned d = cvtpk(bflo(u) - p.mb, bfhi(u) - p.mb);
  return cvtpk(bflo(d) * p.rs, bfhi(d) * p.rs);
}

template <int N1, int N2> __device__ __forceinline__ void throttle(int lvl) {
  asm volatile("s_cmp_lt_i32 %0, 1\n\ts_cbranch_scc1 1f\n\ts_waitcnt vmcnt(%1)\n\ts_cmp_lt_i32 %0, 2\n\ts_cbranch_scc1 1f\n\ts_waitcnt vmcnt(%2)\n1:" :: "s"(lvl), "n"(N1), "n"(N2) : "scc");
}
typedef __attribute__((address_space(3))) unsigned long long lu64;
__device__ __forceinline__ u32x2 lds_ld(const u32x2* p) { return __builtin_bit_cast(u32x2, __hip_atomic_load((lu64*)p, __ATOMIC_RELAXED, __HIP_MEMORY_SCOPE_WORKGROUP)); }
__device__ __forceinline__ void lds_st(u32x2* p, u32x2 v) { __hip_atomic_store((lu64*)p, __builtin_bit_cast(unsigned long long, v), __ATOMIC_RELAXED, __HIP_MEMORY_SCOPE_WORKGROUP); }
template <int V> using IC = std::integral_constant<int, V>;
template <int A, int B, class Fn> __device__ __forceinline__ void sfor(Fn&& f) { if constexpr (A < B) { f(IC<A>{}); sfor<A + 1, B>(f); } }

__global__ void __launch_bounds__(320, 2) fused_decode(const __hip_bfloat16* __restrict__ x, const __hip_bfloat16* __restrict__ kc, const __hip_bfloat16* __restrict__ vc,
                                                    const __hip_bfloat16* __restrict__ wq, const __hip_bfloat16* __restrict__ wo, const __hip_bfloat16* __restrict__ wg,
                                                    const __hip_bfloat16* __restrict__ wu, const __hip_bfloat16* __restrict__ wd, __hip_bfloat16* __restrict__ out, char* ws) {
  __shared__ __attribute__((aligned(16))) float scs[5][NST][64];
  __shared__ __attribute__((aligned(16))) float part[4][128];
  __shared__ __attribute__((aligned(16))) float partf[4][128];
  __shared__ __attribute__((aligned(16))) float ml[4];
  __shared__ __attribute__((aligned(16))) unsigned vecb[64];
  __shared__ __attribute__((aligned(16))) unsigned hb[64];
  __shared__ __attribute__((aligned(16))) u32x2 pace[4][64];
  __shared__ __attribute__((aligned(16))) u32x2 pref;
  const int tid = threadIdx.x, lane = tid & 63, wave = __builtin_amdgcn_readfirstlane(tid >> 6), r = lane >> 4, c = lane & 15;
  const int h = blockIdx.x >> 3, s = blockIdx.x & 7;
  const bool ffn = h < NFB;
  const unsigned lo16 = lane * 16;
  const size_t kvoff = (size_t)h * (SK * DH) + (size_t)s * (SK / 8 * DH);
  asm volatile("" :: "s"(x), "s"(kc), "s"(vc), "s"(wq), "s"(wo), "s"(wg), "s"(wu), "s"(wd), "s"(out), "s"(ws));

  if (__builtin_expect(wave == 4, 0)) {
    __builtin_amdgcn_s_setprio(3);
    const rsrc_t rs = __builtin_amdgcn_make_buffer_rsrc(ws, 0, WS_END, 0x00020000);
    const unsigned long long ep = __hip_atomic_load((gu64*)(ws + WS_EPOCH), __ATOMIC_RELAXED, __HIP_MEMORY_SCOPE_AGENT);
    const unsigned xo = reinterpret_cast<const unsigned*>(x)[64 * s + 2 * h + (lane & 1)];
    const unsigned grpO = WS_O + h * 8192, ownO = grpO + s * 1024, grpP = WS_P + s * (H * 1024), ownP = grpP + h * 1024, grpG = WS_GU + h * 8192, ownG = grpG + s * 1024;
    const unsigned grpQ = WS_Q + h * 4096, ownQ = grpQ + s * 512, grpH = WS_HB + h * 512, ownH = grpH + s * 64, grpM = WS_ML + h * 128, ownM = grpM + s * 16;
    const u32x4 z0 = gload(rs, lo16, ownP), z1 = gload(rs, lo16, ownO);
    const u32x4 z2 = gload(rs, lane < 4 ? (lane * 4 + (h & 3)) * LDK + WS_LD + (h >> 2) * LDH + s * 16 : lane == 4 ? ownM : lane == 5 && ffn ? ownH : lane >= 32 ? ownQ + (lane - 32) * 16 : OOR, 0);
    const u32x4 z3 = gload(rs, ffn ? lo16 : OOR, ownG);
    SB();
    unsigned touch;
    {
      const int hf = ffn ? h : 0, e = lane & 1, w = (lane >> 1) & 7;
      const size_t gofs = (size_t)(256 * (s >> 1) + 255 * e) * F + 128 * hf + 64 * (s & 1), oofs = (size_t)(128 * h + 127 * e) * D + 128 * s, dofs = (size_t)(128 * hf + 127 * e) * D + 128 * s;
      const size_t kofs = kvoff + (size_t)e * (SK / 8 * DH - 64);
      const __hip_bfloat16* p = w == 0 ? kc + kofs : w == 1 ? vc + kofs : w == 2 ? wg + gofs : w == 3 ? wu + gofs : w == 4 ? wo + oofs : wd + dofs;
      touch = __builtin_nontemporal_load(reinterpret_cast<const unsigned*>(p));
      SB();
    }
    const unsigned tA = __builtin_amdgcn_readfirstlane((unsigned)ep) + 1u, tB = __builtin_amdgcn_readfirstlane((unsigned)(ep >> 32)) + 1u;
    auto good = [&](u32x4 g) { return ((g[1] ^ tA) | (g[3] ^ tB)) == 0u; };
    auto sum4 = [&](const float (&p)[4][128], int i) { return (p[0][i] + p[1][i]) + (p[2][i] + p[3][i]); };
    auto sum4x2 = [&](int i) {
      const f32x2 a = *reinterpret_cast<const f32x2*>(&part[0][i]), b = *reinterpret_cast<const f32x2*>(&part[1][i]);
      const f32x2 cc = *reinterpret_cast<const f32x2*>(&part[2][i]), d = *reinterpret_cast<const f32x2*>(&part[3][i]);
      return (a + b) + (cc + d);
    };
    u32x4 P[8];
    pref = u32x2{0u, 0x7fffffffu};
    pace[lane & 3][0] = u32x2{0u, 0u};
    wg_barrier();
    {
      const f32x2 v = sum4x2(2 * (lane & 31));
      gstore(rs, lane < 32 ? lo16 : OOR, ownQ, v[0], v[1], tA, tB);
      SB();
      __builtin_amdgcn_s_sleep(20);
      const bool oh = (lane >> 5) == (s & 1);
      const unsigned voff = (lane >> 5) * 512 + (lane & 31) * 16;
#pragma unroll
      for (int p = 0; p < 4; ++p) P[p] = u32x4{0, 0, 0, 0};
      unsigned miss = 0xfu;
      for (unsigned spin = 0; miss != 0u && spin < SPIN_CAP; ++spin) {
        asm volatile("" ::: "memory");
        u32x4 N[4];
#pragma unroll
        for (int p = 0; p < 4; ++p) { N[p] = gload(rs, (miss >> p & 1u) && !(oh && p == (s >> 1)) ? voff : OOR, grpQ + p * 1024); SB(); }
#pragma unroll
        for (int p = 0; p < 4; ++p) {
          if (oh && p == (s >> 1)) N[p] = u32x4{f2u(v[0]), tA, f2u(v[1]), tB};
          P[p] = (miss >> p & 1u) ? N[p] : P[p]; if (__all(good(N[p]))) miss &= ~(1u << p);
        }
      }
      vecb[lane] = cvtpk((u2f(P[0][0]) + u2f(P[1][0])) + (u2f(P[2][0]) + u2f(P[3][0])), (u2f(P[0][2]) + u2f(P[1][2])) + (u2f(P[2][2]) + u2f(P[3][2])));
    }
    asm volatile("" :: "v"(z0), "v"(z1), "v"(z2), "v"(z3), "v"(touch));
    wg_barrier();
    __builtin_amdgcn_s_setprio(0);
    if (ffn) gstore(rs, lo16, ownG, sum4(partf, lane), sum4(partf, 64 + lane), tA, tB);
    auto wait_for = [&](unsigned voff, unsigned base, auto sl_) {
      u32x4 g;
      for (unsigned spin = 0; spin < SPIN_CAP; ++spin) {
        asm volatile("" ::: "memory");
        g = gload(rs, voff, base);
        if (__all(good(g))) break;
        if (decltype(sl_)::value) __builtin_amdgcn_s_sleep(decltype(sl_)::value);
      }
      return g;
    };
    auto ffn1 = [&]() {
      if (!ffn) return;
      const u32x4 g = wait_for(((s >> 2) + 2 * r) * 1024 + (16 * (s & 3) + c) * 16, grpG, IC<64>{});
      const float gs = rbf(xrow_sum(u2f(g[0]))), us = rbf(xrow_sum(u2f(g[2])));
      const float hv = rbf(1.f / (1.f + __expf(-gs))) * us;
      const unsigned w2 = cvtpk(hv, dpp<XOR1>(hv));
      gstore(rs, (lane & 0x33) == 0 ? (lane >> 2) * 16 : OOR, ownH, u2f(w2), dpp<XOR2>(u2f(w2)), tA, tB);
    };
    auto ffn2 = [&]() {
      if (ffn) {
        const u32x4 g = wait_for((lane & 31) * 16, grpH, IC<64>{});
        if (lane < 32) *reinterpret_cast<u32x2*>(&hb[2 * lane]) = u32x2{g[0], g[2]};
      } else {
        hb[lane] = 0u;
      }
    };
    f32x4 fw;
    auto ml1 = [&]() {
      while (!__all((int)lds_ld(&pace[lane & 3][0])[1] >= NST)) __builtin_amdgcn_s_sleep(16);
      asm volatile("" ::: "memory");
      fw = *reinterpret_cast<const f32x4*>(&ml[0]);
      const float M = fmaxf(fmaxf(fw[0], fw[1]), fmaxf(fw[2], fw[3]));
      const f32x4* sp = reinterpret_cast<const f32x4*>(&scs[0][0][0]) + lane;
      float l0 = 0.f, l1 = 0.f;
#pragma unroll 8
      for (int j = 0; j < NST; ++j) { const f32x4 v = sp[64 * j]; l0 += __builtin_amdgcn_exp2f(v[0] - M); l1 += __builtin_amdgcn_exp2f(v[2] - M); }
      gstore(rs, lane == 0 ? 0u : OOR, ownM, M, wave_sum(l0 + l1), tA, tB);
    };
    auto ml2 = [&]() {
      const u32x4 g = wait_for((lane & 7) * 16, grpM, IC<64>{});
      const float mp = u2f(g[0]);
      float M = fmaxf(mp, dpp<XOR1>(mp)); M = fmaxf(M, dpp<XOR2>(M)); M = fmaxf(M, dpp<XOR7>(M));
      float L = __builtin_amdgcn_exp2f(mp - M) * u2f(g[2]); L += dpp<XOR1>(L); L += dpp<XOR2>(L); L += dpp<XOR7>(L);
#pragma unroll
      for (int w = 0; w < 4; ++w) fw[w] = __builtin_amdgcn_exp2f(fw[w] - M) / L;
    };
    {
      auto reach = [&](int n) { u32x2 pc = lds_ld(&pace[lane & 3][0]);
        while ((int)__builtin_amdgcn_readfirstlane(pc[1]) < n) { __builtin_amdgcn_s_sleep(16); pc = lds_ld(&pace[lane & 3][0]); }
        return pc[0]; };
      for (int n = RFIRST, k = 0; n + RDLY <= RLAST; n += RPER, ++k) {
        const unsigned pc = reach(n);
        const int l0 = __builtin_amdgcn_readfirstlane(pc);
        float dl = (float)((int)pc - l0); dl += dpp<XOR1>(dl); dl += dpp<XOR2>(dl);
        const int my = l0 + (int)(dl * 0.25f);
        if ((h & 3) == (k & 3)) gstore(rs, lane == 0 ? 0u : OOR, WS_LD + k * LDK + (h >> 2) * LDH + s * 16, u2f(my), 0.f, tA, tB);
        reach(n + RDLY);
        const u32x4 g = gload(rs, lane < 32 ? ((((h + 5 * k) >> 2) + 2 * (lane >> 3)) & 7) * LDH + (lane & 7) * 16 : OOR, WS_LD + k * LDK);
        const bool ok = good(g);
        const float sd = wave_sum(ok ? (float)((int)g[0] - my) : 0.f), sn = wave_sum(ok ? 1.f : 0.f);
        if (sn >= 4.f) lds_st(&pref, u32x2{(unsigned)(my + (int)(sd / sn) + BIAS), (unsigned)TH});
        if (k == KG) ffn1();
        if (k == KG + 1) ffn2();
        if (k == KM) ml1();
        if (k == KM + 1) ml2();
      }
    }
    __builtin_amdgcn_s_setprio(3);

    wg_barrier();
    {
      const f32x2 a = *reinterpret_cast<const f32x2*>(&part[0][2 * lane]), b = *reinterpret_cast<const f32x2*>(&part[1][2 * lane]);
      const f32x2 cc = *reinterpret_cast<const f32x2*>(&part[2][2 * lane]), d = *reinterpret_cast<const f32x2*>(&part[3][2 * lane]);
      const f32x2 o = (a * fw[0] + b * fw[1]) + (cc * fw[2] + d * fw[3]);
      gstore(rs, lo16, ownO, o[0], o[1], tA, tB);
#pragma unroll
      for (int p = 0; p < 8; ++p) P[p] = u32x4{f2u(o[0]), tA, f2u(o[1]), tB};
    }
    __builtin_amdgcn_s_sleep(20);
    {
      unsigned miss = 0xffu & ~(1u << s);
      for (unsigned spin = 0; miss != 0u && spin < SPIN_CAP; ++spin) {
        asm volatile("" ::: "memory");
        u32x4 N[8];
#pragma unroll
        for (int p = 0; p < 8; ++p) { N[p] = gload(rs, lo16 | ((miss >> p & 1u) ? 0u : OOR), grpO + p * 1024); SB(); }
#pragma unroll
        for (int p = 0; p < 8; ++p) { P[p] = (miss >> p & 1u) ? N[p] : P[p]; if (__all(good(N[p]))) miss &= ~(1u << p); }
      }
      auto sum8 = [&](int e) { return ((u2f(P[0][e]) + u2f(P[1][e])) + (u2f(P[2][e]) + u2f(P[3][e]))) + ((u2f(P[4][e]) + u2f(P[5][e])) + (u2f(P[6][e]) + u2f(P[7][e]))); };
      vecb[lane] = cvtpk(sum8(0), sum8(2));
    }
    wg_barrier();
    wg_barrier();
    {
      const f32x2 v = sum4x2(2 * lane);
      gstore(rs, lo16, ownP, v[0], v[1], tA, tB);
    }
    __builtin_amdgcn_s_sleep(15);
    {
      const u32x4 g = wait_for((lane >> 1) * 1024 + (2 * h + (lane & 1)) * 16, grpP, IC<0>{});
      auto col_reduce = [&](float v) { v += dpp<XOR2>(v); v += dpp<ROR4>(v); v += dpp<XOR8>(v); return xrow_sum(v); };
      const unsigned w2 = cvtpk(bflo(xo) + col_reduce(u2f(g[0])), bfhi(xo) + col_reduce(u2f(g[2])));
      if (lane < 2) reinterpret_cast<unsigned*>(out)[64 * s + 2 * h + lane] = w2;
    }
    if (blockIdx.x == 0 && lane == 0) __hip_atomic_store((gu64*)(ws + WS_EPOCH), ((unsigned long long)tB << 32) | tA, __ATOMIC_RELAXED, __HIP_MEMORY_SCOPE_AGENT);
    return;
  }

  u32x4 xa = reinterpret_cast<const u32x4*>(x)[2 * lane], xb = reinterpret_cast<const u32x4*>(x)[2 * lane + 1];
  const int row0 = 256 * (s >> 1) + 64 * wave + 8 * (lane >> 3), col0 = 128 * h + 64 * (s & 1) + 8 * (lane & 7);
  u32x4 xr = *reinterpret_cast<const u32x4*>(x + row0);
  SB();
  const rsrc_t rq = mkrs(wq, D * H * DH * 2), ro = mkrs(wo, D * H * DH * 2), rg = mkrs(wg, D * F * 2), ru = mkrs(wu, D * F * 2), rd = mkrs(wd, D * F * 2);
  const rsrc_t rk = mkrs(kc + kvoff, SK / 8 * DH * 2), rv = mkrs(vc + kvoff, SK / 8 * DH * 2);
  const unsigned vq = (row0 * (H * DH) + col0) * 2, vg = ffn ? (row0 * F + col0) * 2 : OOR, vo = ((128 * h + 32 * wave + 8 * r) * D + 128 * s + 8 * c) * 2;
#define KV(RS, I, P) ldnt(RS, lo16 + ((P) & 3) * 1024, (4 * (I) + (wave ^ ((I) & 1))) * 8192 + ((P) >> 2) * 4096)
  u32x4 R[8];
  {
    constexpr int NL = 24;
    u32x4 W[NL];
    auto issue = [&](auto n_) {
      constexpr int n = decltype(n_)::value, i = n & 7, m = n >> 3;
      if constexpr (n >= NL) {
        if constexpr (n >= NL + W0 && n - NL < DHI) asm volatile("s_waitcnt vmcnt(%0)" :: "n"(W0 - 1));
        if constexpr (n - NL < DHI) R[n - NL] = KV(rk, 0, n - NL);
      }
      else if constexpr (m == 0) W[n] = ldnt(rq, vq, i * (H * DH * 2));
      else if constexpr (m == 1) W[n] = ldnt(rg, vg, i * (F * 2));
      else W[n] = ldnt(ru, vg, i * (F * 2));
      SB();
    };
    sfor<0, W0>(issue);
    PIN(xa); PIN(xb); PIN(xr);
    const LN ln = ln_stats(xa, xb);
#pragma unroll
    for (int j = 0; j < 4; ++j) xr[j] = ln_pair(xr[j], ln);
    PIN(xr);
    float a[8];
    sfor<0, NL / 2>([&](auto j_) {
      constexpr int j = decltype(j_)::value, m = j >> 2, jj = j & 3;
      if constexpr (jj == 0) {
#pragma unroll
        for (int k = 0; k < 8; ++k) a[k] = 0.f;
      }
      mac_rows(a, W[2 * j], W[2 * j + 1], xr[jj]); PIN8(a);
      SB();
      issue(IC<W0 + 2 * j>{}); issue(IC<W0 + 2 * j + 1>{});
      if constexpr (jj == 3) {
        const float v = rows8_reduce(a, lane);
        const int col = 8 * (lane & 7) + (lane >> 3);
        if constexpr (m == 0) { part[wave][col] = v; wg_barrier(); }
        else partf[wave][64 * (m - 1) + col] = v;
        SB();
      }
    });
    sfor<W0 + NL, NL + DHI>(issue);
  }
  wg_barrier();
  u32x4 q = *reinterpret_cast<const u32x4*>(&vecb[4 * c]);
  PIN(q);
  int lvl = 1;
  auto pace0 = [&](unsigned long long& t, u32x2& rf) { t = __builtin_amdgcn_s_memrealtime(); rf = lds_ld(&pref); SB(); };
  auto pace1 = [&](int n, unsigned long long t, u32x2 rf) {
    asm volatile("" : "+s"(t));
    const int my = n * TN - (int)t;
    lds_st(&pace[wave][lane], u32x2{(unsigned)my, (unsigned)n});
    const int df = my - (int)__builtin_amdgcn_readfirstlane(rf[0]), th = __builtin_amdgcn_readfirstlane(rf[1]);
    lvl = df < -th ? 0 : df > th ? 2 : 1;
  };

  constexpr float L2E = 1.4426950408889634f;
  const float scb = rbf(0.088388347648318440f);
  float m = -INFINITY;
  auto kstep = [&](int i, auto last_) {
    constexpr bool last = decltype(last_)::value;
    unsigned long long tt; u32x2 rf; pace0(tt, rf);
    float xs[8];
#pragma unroll
    for (int t = 0; t < 8; ++t) {
      float d = dot2(R[t][0], q[0], 0.f); d = dot2(R[t][1], q[1], d); d = dot2(R[t][2], q[2], d); d = dot2(R[t][3], q[3], d);
      xs[t] = d; PIN(xs[t]); SB();
      const int n = t + DHI;
      throttle<DMID - 1, DLO - 1>(lvl);
      SB();
      if (n < 8) R[n] = KV(rk, i, n); else if (!last) R[n - 8] = KV(rk, i + 1, n - 8); else R[n - 8] = KV(rv, 0, n - 8);
      SB();
    }
#define TR_STEP(HALF, CTRL, BIT) _Pragma("unroll") for (int hh = 0; hh < (HALF); ++hh) { \
    const float t1_ = xs[hh] + dpp<CTRL>(xs[hh]), t2_ = xs[hh + (HALF)] + dpp<CTRL>(xs[hh + (HALF)]); xs[hh] = (c & (BIT)) ? t2_ : t1_; }
    TR_STEP(4, XOR8, 8) TR_STEP(2, XOR7, 4) TR_STEP(1, XOR2, 2)
#undef TR_STEP
    const float dotf = xs[0] + dpp<XOR1>(xs[0]);
    const float sc = rbf(rbf(dotf) * scb) * L2E;
    m = fmaxf(m, sc);
    scs[wave][i][lane] = sc;
    pace1(i, tt, rf);
  };
  for (int i = 0; i < NST - 1; ++i) kstep(i, std::false_type{});
  kstep(NST - 1, std::true_type{});
  m = fmaxf(m, dpp<XOR2>(m)); m = fmaxf(m, dpp<XOR7>(m)); m = fmaxf(m, dpp<XOR8>(m));
  m = xrow_max(m);
  ml[wave] = m;
  asm volatile("" ::: "memory");
  f32x2 acc[4] = {{0, 0}, {0, 0}, {0, 0}, {0, 0}};
  u32x4 WT[16];
  auto tissue = [&](int n) { WT[n] = ldnt(n < 8 ? rd : ro, vo, (n & 7) * (D * 2)); };
  auto vstep = [&](int i, auto last_) {
    constexpr bool last = decltype(last_)::value;
    const float p_ = __builtin_amdgcn_exp2f(scs[wave][i][lane] - m);
    SB();
    unsigned long long tt; u32x2 rf; if (!last) pace0(tt, rf);
#define PV(T) { const float pt = dpp<0x150 + 2 * (T)>(p_); const f32x2 p2 = {pt, pt}; \
    _Pragma("unroll") for (int k = 0; k < 4; ++k) acc[k] = __builtin_elementwise_fma(p2, f32x2{bflo(R[T][k]), bfhi(R[T][k])}, acc[k]); \
    PIN(acc[0]); PIN(acc[1]); PIN(acc[2]); PIN(acc[3]); SB(); \
    const int n = (T) + DHI; throttle<DMID - 1, DLO - 1>(lvl); SB(); \
    if (n < 8) R[n & 7] = KV(rv, i, n); else if (!last) R[n & 7] = KV(rv, i + 1, n - 8); else tissue(n - 8); SB(); }
    PV(0) PV(1) PV(2) PV(3) PV(4) PV(5) PV(6) PV(7)
#undef PV
    if (!last) pace1(NST + i, tt, rf);
  };
  for (int i = 0; i < NST - 1; ++i) vstep(i, std::false_type{});
  vstep(NST - 1, std::true_type{});
  {
    const float a[8] = {acc[0][0], acc[0][1], acc[1][0], acc[1][1], acc[2][0], acc[2][1], acc[3][0], acc[3][1]};
    *reinterpret_cast<f32x2*>(&part[wave][8 * c + 2 * r]) = rows_reduce(a);
  }
  wg_barrier();
#pragma unroll
  for (int n = DHI; n < 16; ++n) { asm volatile("s_waitcnt vmcnt(%0)" :: "n"(TWW - 1)); SB(); tissue(n); SB(); }
  unsigned wl[16], wh[16];
  float a[8] = {0, 0, 0, 0, 0, 0, 0, 0};
  auto tperm = [&](int b) {
#pragma unroll
    for (int j = 0; j < 4; ++j)
#pragma unroll
      for (int k = 0; k < 4; ++k) { wl[4 * j + k] = plo(WT[b + 2 * j][k], WT[b + 2 * j + 1][k]); wh[4 * j + k] = phi(WT[b + 2 * j][k], WT[b + 2 * j + 1][k]); PIN(wl[4 * j + k]); PIN(wh[4 * j + k]); }
    SB();
  };
  auto tile = [&](const unsigned* v) {
    const u32x4 ob = *reinterpret_cast<const u32x4*>(v + 16 * wave + 4 * r);
#pragma unroll
    for (int j = 0; j < 4; ++j)
#pragma unroll
      for (int k = 0; k < 4; ++k) { a[2 * k] = dot2(wl[4 * j + k], ob[j], a[2 * k]); a[2 * k + 1] = dot2(wh[4 * j + k], ob[j], a[2 * k + 1]); }
  };
  tperm(0); tile(hb); PIN8(a); SB();
  tperm(8);
  wg_barrier();
  tile(vecb);
  *reinterpret_cast<f32x2*>(&part[wave][8 * c + 2 * r]) = rows_reduce(a);
  wg_barrier();
}

extern "C" void kernel_launch(void* const* d_in, const int* in_sizes, int n_in, void* d_out, int out_size, void* d_ws, size_t ws_size, hipStream_t stream) {
  for (int i = 0; i < n_in; ++i) fprintf(stderr, "PTR in%d %p\n", i, d_in[i]); fprintf(stderr, "PTR out %p ws %p\n", d_out, d_ws);

  fused_decode<<<256, 320, 0, stream>>>((const __hip_bfloat16*)d_in[0], (const __hip_bfloat16*)d_in[1], (const __hip_bfloat16*)d_in[2], (const __hip_bfloat16*)d_in[3],
                                        (const __hip_bfloat16*)d_in[4], (const __hip_bfloat16*)d_in[6], (const __hip_bfloat16*)d_in[7], (const __hip_bfloat16*)d_in[8],
                                        (__hip_bfloat16*)d_out, (char*)d_ws);
}
```

```cpp
#include <hip/hip_runtime.h>
#include <hip/hip_bf16.h>
#include <cstdint>
#include <type_traits>

using bf16x2 = __attribute__((ext_vector_type(2))) __bf16;
using u32x4 = __attribute__((ext_vector_type(4))) unsigned;
using u32x2 = __attribute__((ext_vector_type(2))) unsigned;
using f32x4 = __attribute__((ext_vector_type(4))) float;
using f32x2 = __attribute__((ext_vector_type(2))) float;
typedef __attribute__((address_space(1))) unsigned long long gu64;
typedef __amdgpu_buffer_rsrc_t rsrc_t;

constexpr int D = 1024, H = 32, SK = 65536, DH = 128, F = 3584, NFB = F / 128;
constexpr int NST = SK / 8 / 32 / 4;
constexpr unsigned SPIN_CAP = 1u << 17;
constexpr unsigned OOR = 0x80000000u;

constexpr int DHI = 7, DMID = 5, DLO = 4, W0 = 6, TWW = 5;
constexpr int TN = 106, TH = 50, BIAS = 30, RFIRST = 1, RLAST = 120, RPER = 8, RDLY = 2;
constexpr int KG = 2, KM = (NST - RDLY - RFIRST + RPER - 1) / RPER;

constexpr unsigned CR[2] = {0x111200, 0x9cd00}, CP[3] = {0x19dd00, 0x101200, 0x1efd00}, CW[2] = {0x91400, 0x114a00};
constexpr unsigned CI[6] = {0xbb100, 0xd4400, 0x1bd800, 0x111300, 0x18400, 0x1d4100};
constexpr unsigned CB[8] = {0x734700, 0x4c9800, 0x8e2400, 0x939100, 0x1c5100, 0xc7b00, 0x1efd00, 0x1fd800};
constexpr unsigned CS[8] = {0x3, 0x2, 0x4, 0x80, 0x8, 0x20, 0x60, 0x18};
constexpr unsigned colp(int p) { return (p & 1 ? CP[0] : 0) ^ (p & 2 ? CP[1] : 0) ^ (p & 4 ? CP[2] : 0); }
constexpr unsigned long long cols3(const unsigned (&v)[8], int k, int sh) {
  unsigned long long t = 0;
  for (int i = 0; i < 8; ++i) t |= (unsigned long long)(((i & 1 ? v[k] : 0) ^ (i & 2 ? v[k + 1] : 0) ^ (i & 4 && k + 2 < 8 ? v[k + 2] : 0)) >> sh & 255) << (8 * i);
  return t;
}
template <int N> constexpr bool full_rank(unsigned (&v)[N], unsigned bits) {
  for (int i = 0; i < N; ++i) {
    if (v[i] == 0 || (v[i] & ~bits)) return false;
    for (int j = i + 1; j < N; ++j) if (v[j] & v[i] & (0u - v[i])) v[j] ^= v[i];
  }
  return true;
}
constexpr bool cols_ok() {
  unsigned v[21] = {CR[0], CR[1], CP[0], CP[1], CP[2], CW[0], CW[1], CI[0], CI[1], CI[2], CI[3], CI[4], CI[5], CB[0], CB[1], CB[2], CB[3], CB[4], CB[5], CB[6], CB[7]};
  unsigned u[8] = {CS[0], CS[1], CS[2], CS[3], CS[4], CS[5], CS[6], CS[7]};
  for (int i = 0; i < 21; ++i) if (v[i] & ~(i < 13 ? 0x1fff00u : 0xffff00u)) return false;
  for (int i = 0; i < 8; ++i) v[13 + i] |= u[i] >> 3 << 24;
  return full_rank(u, 0xffu) && full_rank(v, 0x1fffff00u);
}
static_assert(cols_ok() && NST == 64 && H == 32 && DH * 2 == 1 << 8 && SK == 1 << 16);

constexpr unsigned WS_EPOCH = 0;
constexpr unsigned WS_O = 16384;
constexpr unsigned WS_P = WS_O + 256 * 1024;
constexpr unsigned WS_LD = WS_P + 256 * 1024;
constexpr unsigned LDH = 512, LDK = 8 * LDH;
constexpr unsigned WS_GU = WS_LD + 16 * LDK;
constexpr unsigned WS_Q = WS_GU + 256 * 1024;
constexpr unsigned WS_HB = WS_Q + 128 * 1024;
constexpr unsigned WS_ML = WS_HB + 16384;
constexpr unsigned WS_END = WS_ML + 4096;

#define SB() __builtin_amdgcn_sched_barrier(0)
#define PIN(x) asm volatile("" : "+v"(x))
#define PIN8(a) asm volatile("" : "+v"(a[0]), "+v"(a[1]), "+v"(a[2]), "+v"(a[3]), "+v"(a[4]), "+v"(a[5]), "+v"(a[6]), "+v"(a[7]))
__device__ __forceinline__ void wg_barrier() { asm volatile("s_waitcnt lgkmcnt(0)\n\ts_barrier" ::: "memory"); }

__device__ __forceinline__ float dot2(unsigned a, unsigned b, float acc) {
  return __builtin_amdgcn_fdot2_f32_bf16(__builtin_bit_cast(bf16x2, a), __builtin_bit_cast(bf16x2, b), acc, false);
}
__device__ __forceinline__ unsigned cvtpk(float lo, float hi) { return __builtin_bit_cast(unsigned, __builtin_convertvector(f32x2{lo, hi}, bf16x2)); }
__device__ __forceinline__ float bflo(unsigned u) { return __builtin_bit_cast(float, u << 16); }
__device__ __forceinline__ float bfhi(unsigned u) { return __builtin_bit_cast(float, u & 0xffff0000u); }
__device__ __forceinline__ float rbf(float x) { return bflo(cvtpk(x, x)); }
__device__ __forceinline__ float u2f(unsigned u) { return __builtin_bit_cast(float, u); }
__device__ __forceinline__ unsigned f2u(float f) { return __builtin_bit_cast(unsigned, f); }
template <int CTRL> __device__ __forceinline__ float dpp(float x) {
  return __builtin_bit_cast(float, __builtin_amdgcn_mov_dpp(__builtin_bit_cast(int, x), CTRL, 0xf, 0xf, true));
}
constexpr int XOR1 = 0xB1, XOR2 = 0x4E, XOR7 = 0x141, XOR8 = 0x128, ROR4 = 0x124;
__device__ __forceinline__ float swap32_add(float a, float b) {
  auto t = __builtin_amdgcn_permlane32_swap(f2u(a), f2u(b), false, false); return u2f(t[0]) + u2f(t[1]);
}
__device__ __forceinline__ float swap16_add(float a, float b) {
  auto t = __builtin_amdgcn_permlane16_swap(f2u(a), f2u(b), false, false); return u2f(t[0]) + u2f(t[1]);
}
__device__ __forceinline__ float xrow_sum(float x) { x = swap16_add(x, x); return swap32_add(x, x); }
__device__ __forceinline__ float xrow_max(float x) {
  auto s = __builtin_amdgcn_permlane16_swap(f2u(x), f2u(x), false, false); x = fmaxf(u2f(s[0]), u2f(s[1]));
  auto t = __builtin_amdgcn_permlane32_swap(f2u(x), f2u(x), false, false); return fmaxf(u2f(t[0]), u2f(t[1]));
}
__device__ __forceinline__ float row_sum(float x) { x += dpp<XOR1>(x); x += dpp<XOR2>(x); x += dpp<XOR7>(x); x += dpp<XOR8>(x); return x; }
__device__ __forceinline__ float wave_sum(float x) { return xrow_sum(row_sum(x)); }
__device__ __forceinline__ f32x2 rows_reduce(const float (&a)[8]) {
  const float y0 = swap32_add(a[0], a[4]), y1 = swap32_add(a[1], a[5]), y2 = swap32_add(a[2], a[6]), y3 = swap32_add(a[3], a[7]);
  return f32x2{swap16_add(y0, y2), swap16_add(y1, y3)};
}
__device__ __forceinline__ float rows8_reduce(const float (&a)[8], int lane) {
  const f32x2 z = rows_reduce(a);
  const float t1 = z[0] + dpp<XOR8>(z[0]), t2 = z[1] + dpp<XOR8>(z[1]);
  return (lane & 8) ? t2 : t1;
}
__device__ __forceinline__ unsigned plo(unsigned w0, unsigned w1) { return __builtin_amdgcn_perm(w1, w0, 0x05040100u); }
__device__ __forceinline__ unsigned phi(unsigned w0, unsigned w1) { return __builtin_amdgcn_perm(w1, w0, 0x07060302u); }
__device__ __forceinline__ void mac_rows(float (&acc)[8], u32x4 w0, u32x4 w1, unsigned xp) {
#pragma unroll
  for (int k = 0; k < 4; ++k) { acc[2 * k] = dot2(plo(w0[k], w1[k]), xp, acc[2 * k]); acc[2 * k + 1] = dot2(phi(w0[k], w1[k]), xp, acc[2 * k + 1]); }
}
__device__ __forceinline__ rsrc_t mkrs(const void* p, unsigned bytes) { return __builtin_amdgcn_make_buffer_rsrc(const_cast<void*>(p), 0, bytes, 0x00020000); }
__device__ __forceinline__ u32x4 ldnt(rsrc_t rs, unsigned voff, unsigned soff) { return __builtin_amdgcn_raw_buffer_load_b128(rs, voff, soff, 2); }
__device__ __forceinline__ u32x4 gload(rsrc_t rs, unsigned voff, unsigned soff) { return __builtin_amdgcn_raw_buffer_load_b128(rs, voff, soff, 16); }
__device__ __forceinline__ void gstore(rsrc_t rs, unsigned voff, unsigned soff, float v0, float v1, unsigned tA, unsigned tB) {
  __builtin_amdgcn_raw_buffer_store_b128(u32x4{f2u(v0), tA, f2u(v1), tB}, rs, voff + soff, 0, 16);
}
struct LN { float mb, rs; };
__device__ __forceinline__ LN ln_stats(u32x4 a, u32x4 b) {
  float s = 0.f;
#pragma unroll
  for (int k = 0; k < 4; ++k) { s = dot2(a[k], 0x3f803f80u, s); s = dot2(b[k], 0x3f803f80u, s); }
  const float mean = wave_sum(s) * (1.f / D);
  f32x2 q = {0.f, 0.f};
#pragma unroll
  for (int k = 0; k < 4; ++k) {
    const f32x2 da = f32x2{bflo(a[k]), bfhi(a[k])} - mean, db = f32x2{bflo(b[k]), bfhi(b[k])} - mean;
    q = __builtin_elementwise_fma(da, da, q); q = __builtin_elementwise_fma(db, db, q);
  }
  const float var = wave_sum(q[0] + q[1]) * (1.f / D);
  return LN{rbf(mean), __builtin_amdgcn_rcpf(rbf(__builtin_amdgcn_sqrtf(rbf(rbf(var) + 1e-5f))))};
}
__device__ __forceinline__ unsigned ln_pair(unsigned u, LN p) {
  const unsigned d = cvtpk(bflo(u) - p.mb, bfhi(u) - p.mb);
  return cvtpk(bflo(d) * p.rs, bfhi(d) * p.rs);
}

template <int N1, int N2> __device__ __forceinline__ void throttle(int lvl) {
  asm volatile("s_cmp_lt_i32 %0, 1\n\ts_cbranch_scc1 1f\n\ts_waitcnt vmcnt(%1)\n\ts_cmp_lt_i32 %0, 2\n\ts_cbranch_scc1 1f\n\ts_waitcnt vmcnt(%2)\n1:" :: "s"(lvl), "n"(N1), "n"(N2) : "scc");
}
typedef __attribute__((address_space(3))) unsigned long long lu64;
__device__ __forceinline__ u32x2 lds_ld(const u32x2* p) { return __builtin_bit_cast(u32x2, __hip_atomic_load((lu64*)p, __ATOMIC_RELAXED, __HIP_MEMORY_SCOPE_WORKGROUP)); }
__device__ __forceinline__ void lds_st(u32x2* p, u32x2 v) { __hip_atomic_store((lu64*)p, __builtin_bit_cast(unsigned long long, v), __ATOMIC_RELAXED, __HIP_MEMORY_SCOPE_WORKGROUP); }
template <int V> using IC = std::integral_constant<int, V>;
template <int A, int B, class Fn> __device__ __forceinline__ void sfor(Fn&& f) { if constexpr (A < B) { f(IC<A>{}); sfor<A + 1, B>(f); } }

__global__ void __launch_bounds__(320, 2) fused_decode(const __hip_bfloat16* __restrict__ x, const __hip_bfloat16* __restrict__ kc, const __hip_bfloat16* __restrict__ vc,
                                                    const __hip_bfloat16* __restrict__ wq, const __hip_bfloat16* __restrict__ wo, const __hip_bfloat16* __restrict__ wg,
                                                    const __hip_bfloat16* __restrict__ wu, const __hip_bfloat16* __restrict__ wd, __hip_bfloat16* __restrict__ out, char* ws) {
  __shared__ __attribute__((aligned(16))) float scs[5][NST][64];
  __shared__ __attribute__((aligned(16))) float part[4][128];
  __shared__ __attribute__((aligned(16))) float partf[4][128];
  __shared__ __attribute__((aligned(16))) float ml[4];
  __shared__ __attribute__((aligned(16))) unsigned vecb[64];
  __shared__ __attribute__((aligned(16))) unsigned hb[64];
  __shared__ __attribute__((aligned(16))) u32x2 pace[4][64];
  __shared__ __attribute__((aligned(16))) u32x2 pref;
  const int tid = threadIdx.x, lane = tid & 63, wave = __builtin_amdgcn_readfirstlane(tid >> 6), r = lane >> 4, c = lane & 15;
  const int hb_ = blockIdx.x >> 3, sb_ = blockIdx.x & 7;
#define XOR_WG(V, SH) (int)(((cols3(V, 0, SH) >> (8 * sb_)) ^ (cols3(V, 3, SH) >> (8 * (hb_ & 7))) ^ (cols3(V, 6, SH) >> (8 * (hb_ >> 3)))) & 255)
  const int hs = XOR_WG(CS, 0), h = hs >> 3, s = hs & 7;
  const bool ffn = h < NFB;
  const unsigned lo16 = lane * 16;
  const size_t kvoff = (size_t)h * (SK * DH);
  asm volatile("" :: "s"(x), "s"(kc), "s"(vc), "s"(wq), "s"(wo), "s"(wg), "s"(wu), "s"(wd), "s"(out), "s"(ws));

  if (__builtin_expect(wave == 4, 0)) {
    __builtin_amdgcn_s_setprio(3);
    const rsrc_t rs = __builtin_amdgcn_make_buffer_rsrc(ws, 0, WS_END, 0x00020000);
    const unsigned long long ep = __hip_atomic_load((gu64*)(ws + WS_EPOCH), __ATOMIC_RELAXED, __HIP_MEMORY_SCOPE_AGENT);
    const unsigned xo = reinterpret_cast<const unsigned*>(x)[64 * s + 2 * h + (lane & 1)];
    const unsigned grpO = WS_O + h * 8192, ownO = grpO + s * 1024, grpP = WS_P + s * (H * 1024), ownP = grpP + h * 1024, grpG = WS_GU + h * 8192, ownG = grpG + s * 1024;
    const unsigned grpQ = WS_Q + h * 4096, ownQ = grpQ + s * 512, grpH = WS_HB + h * 512, ownH = grpH + s * 64, grpM = WS_ML + h * 128, ownM = grpM + s * 16;
    const u32x4 z0 = gload(rs, lo16, ownP), z1 = gload(rs, lo16, ownO);
    const u32x4 z2 = gload(rs, lane < 4 ? (lane * 4 + (hb_ & 3)) * LDK + WS_LD + (hb_ >> 2) * LDH + sb_ * 16 : lane == 4 ? ownM : lane == 5 && ffn ? ownH : lane >= 32 ? ownQ + (lane - 32) * 16 : OOR, 0);
    const u32x4 z3 = gload(rs, ffn ? lo16 : OOR, ownG);
    SB();
    unsigned touch;
    {
      const int hf = ffn ? h : 0, e = lane & 1, w = (lane >> 1) & 7;
      const size_t gofs = (size_t)(256 * (s >> 1) + 255 * e) * F + 128 * hf + 64 * (s & 1), oofs = (size_t)(128 * h + 127 * e) * D + 128 * s, dofs = (size_t)(128 * hf + 127 * e) * D + 128 * s;
      const size_t kofs = kvoff + (size_t)XOR_WG(CB, 21) * (SK / 8 * DH) + (size_t)e * (SK / 8 * DH - 64);
      const __hip_bfloat16* p = w == 0 ? kc + kofs : w == 1 ? vc + kofs : w == 2 ? wg + gofs : w == 3 ? wu + gofs : w == 4 ? wo + oofs : w == 6 ? out + 128 * s + 4 * h : wd + dofs;
      touch = __builtin_nontemporal_load(reinterpret_cast<const unsigned*>(p));
      SB();
    }
    const unsigned tA = __builtin_amdgcn_readfirstlane((unsigned)ep) + 1u, tB = __builtin_amdgcn_readfirstlane((unsigned)(ep >> 32)) + 1u;
    auto good = [&](u32x4 g) { return ((g[1] ^ tA) | (g[3] ^ tB)) == 0u; };
    auto sum4 = [&](const float (&p)[4][128], int i) { return (p[0][i] + p[1][i]) + (p[2][i] + p[3][i]); };
    auto sum4x2 = [&](int i) {
      const f32x2 a = *reinterpret_cast<const f32x2*>(&part[0][i]), b = *reinterpret_cast<const f32x2*>(&part[1][i]);
      const f32x2 cc = *reinterpret_cast<const f32x2*>(&part[2][i]), d = *reinterpret_cast<const f32x2*>(&part[3][i]);
      return (a + b) + (cc + d);
    };
    u32x4 P[8];
    pref = u32x2{0u, 0x7fffffffu};
    pace[lane & 3][0] = u32x2{0u, 0u};
    wg_barrier();
    {
      const f32x2 v = sum4x2(2 * (lane & 31));
      gstore(rs, lane < 32 ? lo16 : OOR, ownQ, v[0], v[1], tA, tB);
      SB();
      __builtin_amdgcn_s_sleep(20);
      const bool oh = (lane >> 5) == (s & 1);
      const unsigned voff = (lane >> 5) * 512 + (lane & 31) * 16;
#pragma unroll
      for (int p = 0; p < 4; ++p) P[p] = u32x4{0, 0, 0, 0};
      unsigned miss = 0xfu;
      for (unsigned spin = 0; miss != 0u && spin < SPIN_CAP; ++spin) {
        asm volatile("" ::: "memory");
        u32x4 N[4];
#pragma unroll
        for (int p = 0; p < 4; ++p) { N[p] = gload(rs, (miss >> p & 1u) && !(oh && p == (s >> 1)) ? voff : OOR, grpQ + p * 1024); SB(); }
#pragma unroll
        for (int p = 0; p < 4; ++p) {
          if (oh && p == (s >> 1)) N[p] = u32x4{f2u(v[0]), tA, f2u(v[1]), tB};
          P[p] = (miss >> p & 1u) ? N[p] : P[p]; if (__all(good(N[p]))) miss &= ~(1u << p);
        }
      }
      vecb[lane] = cvtpk((u2f(P[0][0]) + u2f(P[1][0])) + (u2f(P[2][0]) + u2f(P[3][0])), (u2f(P[0][2]) + u2f(P[1][2])) + (u2f(P[2][2]) + u2f(P[3][2])));
    }
    asm volatile("" :: "v"(z0), "v"(z1), "v"(z2), "v"(z3), "v"(touch));
    wg_barrier();
    __builtin_amdgcn_s_setprio(0);
    if (ffn) gstore(rs, lo16, ownG, sum4(partf, lane), sum4(partf, 64 + lane), tA, tB);
    auto wait_for = [&](unsigned voff, unsigned base, auto sl_) {
      u32x4 g;
      for (unsigned spin = 0; spin < SPIN_CAP; ++spin) {
        asm volatile("" ::: "memory");
        g = gload(rs, voff, base);
        if (__all(good(g))) break;
        if (decltype(sl_)::value) __builtin_amdgcn_s_sleep(decltype(sl_)::value);
      }
      return g;
    };
    auto ffn1 = [&]() {
      if (!ffn) return;
      const u32x4 g = wait_for(((s >> 2) + 2 * r) * 1024 + (16 * (s & 3) + c) * 16, grpG, IC<64>{});
      const float gs = rbf(xrow_sum(u2f(g[0]))), us = rbf(xrow_sum(u2f(g[2])));
      const float hv = rbf(1.f / (1.f + __expf(-gs))) * us;
      const unsigned w2 = cvtpk(hv, dpp<XOR1>(hv));
      gstore(rs, (lane & 0x33) == 0 ? (lane >> 2) * 16 : OOR, ownH, u2f(w2), dpp<XOR2>(u2f(w2)), tA, tB);
    };
    auto ffn2 = [&]() {
      if (ffn) {
        const u32x4 g = wait_for((lane & 31) * 16, grpH, IC<64>{});
        if (lane < 32) *reinterpret_cast<u32x2*>(&hb[2 * lane]) = u32x2{g[0], g[2]};
      } else {
        hb[lane] = 0u;
      }
    };
    f32x4 fw;
    auto ml1 = [&]() {
      while (!__all((int)lds_ld(&pace[lane & 3][0])[1] >= NST)) __builtin_amdgcn_s_sleep(16);
      asm volatile("" ::: "memory");
      fw = *reinterpret_cast<const f32x4*>(&ml[0]);
      const float M = fmaxf(fmaxf(fw[0], fw[1]), fmaxf(fw[2], fw[3]));
      const f32x4* sp = reinterpret_cast<const f32x4*>(&scs[0][0][0]) + lane;
      float l0 = 0.f, l1 = 0.f;
#pragma unroll 8
      for (int j = 0; j < NST; ++j) { const f32x4 v = sp[64 * j]; l0 += __builtin_amdgcn_exp2f(v[0] - M); l1 += __builtin_amdgcn_exp2f(v[2] - M); }
      gstore(rs, lane == 0 ? 0u : OOR, ownM, M, wave_sum(l0 + l1), tA, tB);
    };
    auto ml2 = [&]() {
      const u32x4 g = wait_for((lane & 7) * 16, grpM, IC<64>{});
      const float mp = u2f(g[0]);
      float M = fmaxf(mp, dpp<XOR1>(mp)); M = fmaxf(M, dpp<XOR2>(M)); M = fmaxf(M, dpp<XOR7>(M));
      float L = __builtin_amdgcn_exp2f(mp - M) * u2f(g[2]); L += dpp<XOR1>(L); L += dpp<XOR2>(L); L += dpp<XOR7>(L);
#pragma unroll
      for (int w = 0; w < 4; ++w) fw[w] = __builtin_amdgcn_exp2f(fw[w] - M) / L;
    };
    {
      auto reach = [&](int n) { u32x2 pc = lds_ld(&pace[lane & 3][0]);
        while ((int)__builtin_amdgcn_readfirstlane(pc[1]) < n) { __builtin_amdgcn_s_sleep(16); pc = lds_ld(&pace[lane & 3][0]); }
        return pc[0]; };
      for (int n = RFIRST, k = 0; n + RDLY <= RLAST; n += RPER, ++k) {
        const unsigned pc = reach(n);
        const int l0 = __builtin_amdgcn_readfirstlane(pc);
        float dl = (float)((int)pc - l0); dl += dpp<XOR1>(dl); dl += dpp<XOR2>(dl);
        const int my = l0 + (int)(dl * 0.25f);
        if ((hb_ & 3) == (k & 3)) gstore(rs, lane == 0 ? 0u : OOR, WS_LD + k * LDK + (hb_ >> 2) * LDH + sb_ * 16, u2f(my), 0.f, tA, tB);
        reach(n + RDLY);
        const u32x4 g = gload(rs, lane < 32 ? ((((hb_ + 5 * k) >> 2) + 2 * (lane >> 3)) & 7) * LDH + (lane & 7) * 16 : OOR, WS_LD + k * LDK);
        const bool ok = good(g);
        const float sd = wave_sum(ok ? (float)((int)g[0] - my) : 0.f), sn = wave_sum(ok ? 1.f : 0.f);
        if (sn >= 4.f) lds_st(&pref, u32x2{(unsigned)(my + (int)(sd / sn) + BIAS), (unsigned)TH});
        if (k == KG) ffn1();
        if (k == KG + 1) ffn2();
        if (k == KM) ml1();
        if (k == KM + 1) ml2();
      }
    }
    __builtin_amdgcn_s_setprio(3);

    wg_barrier();
    {
      const f32x2 a = *reinterpret_cast<const f32x2*>(&part[0][2 * lane]), b = *reinterpret_cast<const f32x2*>(&part[1][2 * lane]);
      const f32x2 cc = *reinterpret_cast<const f32x2*>(&part[2][2 * lane]), d = *reinterpret_cast<const f32x2*>(&part[3][2 * lane]);
      const f32x2 o = (a * fw[0] + b * fw[1]) + (cc * fw[2] + d * fw[3]);
      gstore(rs, lo16, ownO, o[0], o[1], tA, tB);
#pragma unroll
      for (int p = 0; p < 8; ++p) P[p] = u32x4{f2u(o[0]), tA, f2u(o[1]), tB};
    }
    __builtin_amdgcn_s_sleep(20);
    {
      unsigned miss = 0xffu & ~(1u << s);
      for (unsigned spin = 0; miss != 0u && spin < SPIN_CAP; ++spin) {
        asm volatile("" ::: "memory");
        u32x4 N[8];
#pragma unroll
        for (int p = 0; p < 8; ++p) { N[p] = gload(rs, lo16 | ((miss >> p & 1u) ? 0u : OOR), grpO + p * 1024); SB(); }
#pragma unroll
        for (int p = 0; p < 8; ++p) { P[p] = (miss >> p & 1u) ? N[p] : P[p]; if (__all(good(N[p]))) miss &= ~(1u << p); }
      }
      auto sum8 = [&](int e) { return ((u2f(P[0][e]) + u2f(P[1][e])) + (u2f(P[2][e]) + u2f(P[3][e]))) + ((u2f(P[4][e]) + u2f(P[5][e])) + (u2f(P[6][e]) + u2f(P[7][e]))); };
      vecb[lane] = cvtpk(sum8(0), sum8(2));
    }
    wg_barrier();
    wg_barrier();
    {
      const f32x2 v = sum4x2(2 * lane);
      gstore(rs, lo16, ownP, v[0], v[1], tA, tB);
    }
    __builtin_amdgcn_s_sleep(15);
    {
      const u32x4 g = wait_for((lane >> 1) * 1024 + (2 * h + (lane & 1)) * 16, grpP, IC<0>{});
      auto col_reduce = [&](float v) { v += dpp<XOR2>(v); v += dpp<ROR4>(v); v += dpp<XOR8>(v); return xrow_sum(v); };
      const unsigned w2 = cvtpk(bflo(xo) + col_reduce(u2f(g[0])), bfhi(xo) + col_reduce(u2f(g[2])));
      if (lane < 2) reinterpret_cast<unsigned*>(out)[64 * s + 2 * h + lane] = w2;
    }
    if (blockIdx.x == 0 && lane == 0) __hip_atomic_store((gu64*)(ws + WS_EPOCH), ((unsigned long long)tB << 32) | tA, __ATOMIC_RELAXED, __HIP_MEMORY_SCOPE_AGENT);
    return;
  }

  u32x4 xa = reinterpret_cast<const u32x4*>(x)[2 * lane], xb = reinterpret_cast<const u32x4*>(x)[2 * lane + 1];
  const int row0 = 256 * (s >> 1) + 64 * wave + 8 * (lane >> 3), col0 = 128 * h + 64 * (s & 1) + 8 * (lane & 7);
  u32x4 xr = *reinterpret_cast<const u32x4*>(x + row0);
  SB();
  const rsrc_t rq = mkrs(wq, D * H * DH * 2), ro = mkrs(wo, D * H * DH * 2), rg = mkrs(wg, D * F * 2), ru = mkrs(wu, D * F * 2), rd = mkrs(wd, D * F * 2);
  const rsrc_t rk = mkrs(kc + kvoff, SK * DH * 2), rv = mkrs(vc + kvoff, SK * DH * 2);
  const unsigned vq = (row0 * (H * DH) + col0) * 2, vg = ffn ? (row0 * F + col0) * 2 : OOR, vo = ((128 * h + 32 * wave + 8 * r) * D + 128 * s + 8 * c) * 2;
  unsigned ostep = 0, ocur = 0, onxt = 0;
  unsigned olane = c * 16 ^ (r & 1 ? CR[0] : 0) ^ (r & 2 ? CR[1] : 0);
  PIN(olane);
#define KV(RS, O, P) ldnt(RS, olane ^ ((O) ^ colp(P)), 0)
  u32x4 R[8];
  {
    constexpr int NL = 24;
    u32x4 W[NL];
    auto issue = [&](auto n_) {
      constexpr int n = decltype(n_)::value, i = n & 7, m = n >> 3;
      if constexpr (n >= NL) {
        if constexpr (n >= NL + W0 && n - NL < DHI) asm volatile("s_waitcnt vmcnt(%0)" :: "n"(W0 - 1));
        if constexpr (n - NL < DHI) R[n - NL] = KV(rk, onxt, n - NL);
      }
      else if constexpr (m == 0) W[n] = ldnt(rq, vq, i * (H * DH * 2));
      else if constexpr (m == 1) W[n] = ldnt(rg, vg, i * (F * 2));
      else W[n] = ldnt(ru, vg, i * (F * 2));
      SB();
    };
    sfor<0, W0>(issue);
    onxt = (wave & 1 ? CW[0] : 0) ^ (wave & 2 ? CW[1] : 0);
#pragma unroll
    for (int k = 0; k < 8; ++k) onxt ^= (blockIdx.x >> k & 1) ? CB[k] : 0;
#pragma unroll
    for (int k = 0; k < 6; ++k) ostep ^= (lane >> k & 1) ? CI[k] : 0;
    ostep ^= onxt;
    PIN(ostep); asm volatile("" : "+s"(onxt)); SB();
    PIN(xa); PIN(xb); PIN(xr);
    const LN ln = ln_stats(xa, xb);
#pragma unroll
    for (int j = 0; j < 4; ++j) xr[j] = ln_pair(xr[j], ln);
    PIN(xr);
    float a[8];
    sfor<0, NL / 2>([&](auto j_) {
      constexpr int j = decltype(j_)::value, m = j >> 2, jj = j & 3;
      if constexpr (jj == 0) {
#pragma unroll
        for (int k = 0; k < 8; ++k) a[k] = 0.f;
      }
      mac_rows(a, W[2 * j], W[2 * j + 1], xr[jj]); PIN8(a);
      SB();
      issue(IC<W0 + 2 * j>{}); issue(IC<W0 + 2 * j + 1>{});
      if constexpr (jj == 3) {
        const float v = rows8_reduce(a, lane);
        const int col = 8 * (lane & 7) + (lane >> 3);
        if constexpr (m == 0) { part[wave][col] = v; wg_barrier(); }
        else partf[wave][64 * (m - 1) + col] = v;
        SB();
      }
    });
    sfor<W0 + NL, NL + DHI>(issue);
  }
  wg_barrier();
  u32x4 q = *reinterpret_cast<const u32x4*>(&vecb[4 * c]);
  PIN(q);
  int lvl = 1;
  auto pace0 = [&](unsigned long long& t, u32x2& rf) { t = __builtin_amdgcn_s_memrealtime(); rf = lds_ld(&pref); SB(); };
  auto pace1 = [&](int n, unsigned long long t, u32x2 rf) {
    asm volatile("" : "+s"(t));
    const int my = n * TN - (int)t;
    lds_st(&pace[wave][lane], u32x2{(unsigned)my, (unsigned)n});
    const int df = my - (int)__builtin_amdgcn_readfirstlane(rf[0]), th = __builtin_amdgcn_readfirstlane(rf[1]);
    lvl = df < -th ? 0 : df > th ? 2 : 1;
  };

  constexpr float L2E = 1.4426950408889634f;
  const float scb = rbf(0.088388347648318440f);
  float m = -INFINITY;
  auto kstep = [&](int i, auto last_) {
    constexpr bool last = decltype(last_)::value;
    ocur = onxt; onxt = __builtin_amdgcn_readlane(ostep, (i + 1) & (NST - 1));
    unsigned long long tt; u32x2 rf; pace0(tt, rf);
    float xs[8];
#pragma unroll
    for (int t = 0; t < 8; ++t) {
      float d = dot2(R[t][0], q[0], 0.f); d = dot2(R[t][1], q[1], d); d = dot2(R[t][2], q[2], d); d = dot2(R[t][3], q[3], d);
      xs[t] = d; PIN(xs[t]); SB();
      const int n = t + DHI;
      throttle<DMID - 1, DLO - 1>(lvl);
      SB();
      if (n < 8) R[n] = KV(rk, ocur, n); else R[n - 8] = KV(last ? rv : rk, onxt, n - 8);
      SB();
    }
#define TR_STEP(HALF, CTRL, BIT) _Pragma("unroll") for (int hh = 0; hh < (HALF); ++hh) { \
    const float t1_ = xs[hh] + dpp<CTRL>(xs[hh]), t2_ = xs[hh + (HALF)] + dpp<CTRL>(xs[hh + (HALF)]); xs[hh] = (c & (BIT)) ? t2_ : t1_; }
    TR_STEP(4, XOR8, 8) TR_STEP(2, XOR7, 4) TR_STEP(1, XOR2, 2)
#undef TR_STEP
    const float dotf = xs[0] + dpp<XOR1>(xs[0]);
    const float sc = rbf(rbf(dotf) * scb) * L2E;
    m = fmaxf(m, sc);
    scs[wave][i][lane] = sc;
    pace1(i, tt, rf);
  };
  for (int i = 0; i < NST - 1; ++i) kstep(i, std::false_type{});
  kstep(NST - 1, std::true_type{});
  m = fmaxf(m, dpp<XOR2>(m)); m = fmaxf(m, dpp<XOR7>(m)); m = fmaxf(m, dpp<XOR8>(m));
  m = xrow_max(m);
  ml[wave] = m;
  asm volatile("" ::: "memory");
  f32x2 acc[4] = {{0, 0}, {0, 0}, {0, 0}, {0, 0}};
  u32x4 WT[16];
  auto tissue = [&](int n) { WT[n] = ldnt(n < 8 ? rd : ro, vo, (n & 7) * (D * 2)); };
  auto vstep = [&](int i, auto last_) {
    constexpr bool last = decltype(last_)::value;
    ocur = onxt; onxt = __builtin_amdgcn_readlane(ostep, (i + 1) & (NST - 1));
    const float p_ = __builtin_amdgcn_exp2f(scs[wave][i][lane] - m);
    SB();
    unsigned long long tt; u32x2 rf; if (!last) pace0(tt, rf);
#define PV(T) { const float pt = dpp<0x150 + 2 * (T)>(p_); const f32x2 p2 = {pt, pt}; \
    _Pragma("unroll") for (int k = 0; k < 4; ++k) acc[k] = __builtin_elementwise_fma(p2, f32x2{bflo(R[T][k]), bfhi(R[T][k])}, acc[k]); \
    PIN(acc[0]); PIN(acc[1]); PIN(acc[2]); PIN(acc[3]); SB(); \
    const int n = (T) + DHI; throttle<DMID - 1, DLO - 1>(lvl); SB(); \
    if (n < 8) R[n & 7] = KV(rv, ocur, n); else if (!last) R[n & 7] = KV(rv, onxt, n - 8); else tissue(n - 8); SB(); }
    PV(0) PV(1) PV(2) PV(3) PV(4) PV(5) PV(6) PV(7)
#undef PV
    if (!last) pace1(NST + i, tt, rf);
  };
  for (int i = 0; i < NST - 1; ++i) vstep(i, std::false_type{});
  vstep(NST - 1, std::true_type{});
  {
    const float a[8] = {acc[0][0], acc[0][1], acc[1][0], acc[1][1], acc[2][0], acc[2][1], acc[3][0], acc[3][1]};
    *reinterpret_cast<f32x2*>(&part[wave][8 * c + 2 * r]) = rows_reduce(a);
  }
  wg_barrier();
#pragma unroll
  for (int n = DHI; n < 16; ++n) { asm volatile("s_waitcnt vmcnt(%0)" :: "n"(TWW - 1)); SB(); tissue(n); SB(); }
  unsigned wl[16], wh[16];
  float a[8] = {0, 0, 0, 0, 0, 0, 0, 0};
  auto tperm = [&](int b) {
#pragma unroll
    for (int j = 0; j < 4; ++j)
#pragma unroll
      for (int k = 0; k < 4; ++k) { wl[4 * j + k] = plo(WT[b + 2 * j][k], WT[b + 2 * j + 1][k]); wh[4 * j + k] = phi(WT[b + 2 * j][k], WT[b + 2 * j + 1][k]); PIN(wl[4 * j + k]); PIN(wh[4 * j + k]); }
    SB();
  };
  auto tile = [&](const unsigned* v) {
    const u32x4 ob = *reinterpret_cast<const u32x4*>(v + 16 * wave + 4 * r);
#pragma unroll
    for (int j = 0; j < 4; ++j)
#pragma unroll
      for (int k = 0; k < 4; ++k) { a[2 * k] = dot2(wl[4 * j + k], ob[j], a[2 * k]); a[2 * k + 1] = dot2(wh[4 * j + k], ob[j], a[2 * k + 1]); }
  };
  tperm(0); tile(hb); PIN8(a); SB();
  tperm(8);
  wg_barrier();
  tile(vecb);
  *reinterpret_cast<f32x2*>(&part[wave][8 * c + 2 * r]) = rows_reduce(a);
  wg_barrier();
}

extern "C" void kernel_launch(void* const* d_in, const int* in_sizes, int n_in, void* d_out, int out_size, void* d_ws, size_t ws_size, hipStream_t stream) {
  fused_decode<<<256, 320, 0, stream>>>((const __hip_bfloat16*)d_in[0], (const __hip_bfloat16*)d_in[1], (const __hip_bfloat16*)d_in[2], (const __hip_bfloat16*)d_in[3],
                                        (const __hip_bfloat16*)d_in[4], (const __hip_bfloat16*)d_in[6], (const __hip_bfloat16*)d_in[7], (const __hip_bfloat16*)d_in[8],
                                        (__hip_bfloat16*)d_out, (char*)d_ws);
}
```
